```python
import torch

def _softmax(x, axis=-1):
    return torch.softmax(x, dim=axis)

def _gelu(x):
    return torch.nn.functional.gelu(x, approximate='tanh')

def _silu(x):
    return torch.nn.functional.silu(x)

def _sigmoid(x):
    return torch.sigmoid(x)

def _layernorm(x, axis=-1, eps=1e-05):
    m = x.mean(dim=axis, keepdim=True)
    v = x.var(dim=axis, keepdim=True, unbiased=False)
    return (x - m) / torch.sqrt(v + eps)

def _scatter_add(tgt, idx, src, axis=0):
    return tgt.clone().index_add_(axis, idx, src)

def _segment_sum(src, seg, n):
    out = torch.zeros((n,) + src.shape[1:], dtype=src.dtype)
    return out.index_add_(0, seg, src)

def _pad(x, pw):
    flat = tuple((v for pair in reversed(pw) for v in pair))
    return torch.nn.functional.pad(x, flat)
kernel_name = 'gated_mix__memory_aug_recurrent_mem_tr__dense_activation_silu__prompt'

def setup_inputs():

    _w = lambda a: (lambda t: t.to(torch.bfloat16) if t.is_floating_point() else t)(torch.as_tensor(a))

    def _A_setup():
        B, S, Dm = (1, 2048, 512)
        H, Dh = (32, 128)
        M, seg = (8, 32)
        seg = min(seg, S)
        S = S // seg * seg
        return {'x': _w((lambda *s: torch.randn(*s, dtype=torch.bfloat16) * 0.05)(B, S, Dm)), 'mem0': _w((lambda *s: torch.randn(*s, dtype=torch.bfloat16) * 0.4)(B, M, Dm)), 'Wq': _w((lambda *s: torch.randn(*s, dtype=torch.bfloat16) * 6.4)(Dm, H, Dh) * Dm ** (-0.5)), 'Wk': _w((lambda *s: torch.randn(*s, dtype=torch.bfloat16) * 6.4)(Dm, H, Dh) * Dm ** (-0.5)), 'Wv': _w((lambda *s: torch.randn(*s, dtype=torch.bfloat16) * 0.4)(Dm, H, Dh) * Dm ** (-0.5)), 'Wo': _w((lambda *s: torch.randn(*s, dtype=torch.bfloat16) * 0.4)(H, Dh, Dm) * (H * Dh) ** (-0.5)), 'seg_len': seg}
    _A_in = _A_setup()

    def _B_setup():
        B, S, D, F = (1, 2048, 512, 1792)
        return {'x': _w((lambda *s: torch.randn(*s, dtype=torch.bfloat16) * 0.05)(B, S, D)), 'w1': _w((lambda *s: torch.randn(*s, dtype=torch.bfloat16) * 3.2)(D, F) * D ** (-0.5)), 'w2': _w((lambda *s: torch.randn(*s, dtype=torch.bfloat16) * 0.2)(F, D) * F ** (-0.5))}
    _B_in = _B_setup()
    return {'A_x': _A_in['x'], 'A_mem0': _A_in['mem0'], 'A_Wq': _A_in['Wq'], 'A_Wk': _A_in['Wk'], 'A_Wv': _A_in['Wv'], 'A_Wo': _A_in['Wo'], 'A_seg_len': _A_in['seg_len'], 'B_x': _B_in['x'], 'B_w1': _B_in['w1'], 'B_w2': _B_in['w2'], 'gate_w': _w((lambda *s: torch.randn(*s, dtype=torch.bfloat16) * 0.05)(512) * 16.0)}

def reference(A_x, A_mem0, A_Wq, A_Wk, A_Wv, A_Wo, A_seg_len, B_x, B_w1, B_w2, gate_w):

    def _A_ref(x, mem0, Wq, Wk, Wv, Wo, seg_len):
        B, S, Dm = x.shape
        H, Dh = (Wq.shape[1], Wq.shape[2])
        M = mem0.shape[1]
        scale = Dh ** (-0.5)
        n_seg = S // seg_len
        L = M + seg_len
        idx = torch.arange(L)
        mask = (idx[:, None] < M) | (idx[:, None] >= idx[None, :])
        mem = mem0
        outs = []
        for s in range(n_seg):
            xs = x[:, s * seg_len:(s + 1) * seg_len, :].clone()
            h = torch.concatenate([mem, xs], axis=1)
            q = torch.einsum('btd,dhe->bhte', h, Wq)
            k = torch.einsum('btd,dhe->bhte', h, Wk)
            v = torch.einsum('btd,dhe->bhte', h, Wv)
            scores = torch.einsum('bhqe,bhke->bhqk', q, k) * scale
            scores = torch.where(mask[None, None], scores, -1000000000.0)
            p = _softmax(scores, axis=-1)
            o = torch.einsum('bhqk,bhke->bhqe', p, v)
            o = torch.einsum('bhte,hed->btd', o, Wo)
            mem = o[:, :M, :].clone()
            outs.append(o[:, M:, :])
        return torch.concatenate(outs, axis=1)

    def _B_ref(x, w1, w2):
        h = torch.einsum('bsd,df->bsf', x, w1)
        h = _silu(h)
        out = torch.einsum('bsf,fd->bsd', h, w2)
        return out
    x = A_x
    g = _sigmoid((x * gate_w).sum(-1, keepdims=True))
    a = _A_ref(x, A_mem0, A_Wq, A_Wk, A_Wv, A_Wo, A_seg_len)
    b = _B_ref(x, B_w1, B_w2)
    return g * a + (1 - g) * b
```

```cpp
#include <hip/hip_runtime.h>
#include <hip/hip_bf16.h>
#include <stdint.h>

typedef __bf16 bf16;
typedef __bf16 bf16x2 __attribute__((ext_vector_type(2)));
typedef __bf16 bf16x4 __attribute__((ext_vector_type(4)));
typedef __bf16 bf16x8 __attribute__((ext_vector_type(8)));
typedef __bf16 bf16x16 __attribute__((ext_vector_type(16)));
typedef short s16x4 __attribute__((ext_vector_type(4)));
typedef float f32x2 __attribute__((ext_vector_type(2)));
typedef float f32x4 __attribute__((ext_vector_type(4)));
typedef float f32x16 __attribute__((ext_vector_type(16)));
typedef unsigned u32x2 __attribute__((ext_vector_type(2)));
typedef unsigned u32x4 __attribute__((ext_vector_type(4)));
#define LAS __attribute__((address_space(3)))
#define GAS __attribute__((address_space(1)))
typedef LAS unsigned char* ldsp;
#define DEV __device__ __forceinline__

constexpr int S_TOK = 2048, DM = 512, NH = 32, DH = 128, NMEM = 8, SEG = 32, NSEG = 64, FF = 1792;
constexpr int HD = NH * DH;

constexpr size_t WS_HOP = 0;
constexpr size_t WS_QX = 6u << 20;
constexpr size_t WS_KX = WS_QX + (16u << 20);
constexpr size_t WS_VX = WS_KX + (16u << 20);
constexpr size_t WS_KM = WS_VX + (16u << 20);
constexpr size_t WS_VM = WS_KM + (4u << 20);
constexpr size_t WS_O  = WS_VM + (4u << 20);
constexpr size_t WS_H  = WS_O + (16u << 20);
constexpr size_t WS_G  = WS_H + (8u << 20);
constexpr size_t WS_END = WS_G + (1u << 20);
constexpr int C_EPOCH = 0, C_ABORT = 32, C_CAL = 64, C_INIT = 128;
constexpr int C_SLOT = 512;
constexpr int C_PP = C_SLOT + 256 * 16;
constexpr int C_PG = C_PP + 56 * 32;
constexpr int C_PA = C_PG + 8 * 32;
constexpr int C_PM = C_PA + 64 * 32;
constexpr int CTRL_BYTES = 65536;
static_assert((C_PM + 64 * 32) * 4 <= CTRL_BYTES, "control block");

DEV unsigned xcc_id() { return (unsigned)__builtin_amdgcn_s_getreg((3 << 11) | 20) & 0xFu; }
DEV unsigned ld_agent(unsigned* p) { return __hip_atomic_load(p, __ATOMIC_RELAXED, __HIP_MEMORY_SCOPE_AGENT); }
DEV void st_agent(unsigned* p, unsigned v) { __hip_atomic_store(p, v, __ATOMIC_RELAXED, __HIP_MEMORY_SCOPE_AGENT); }
DEV void st_flag(unsigned* p, unsigned v, unsigned tag) { __hip_atomic_store((uint64_t*)p, ((uint64_t)tag << 32) | v, __ATOMIC_RELAXED, __HIP_MEMORY_SCOPE_AGENT); }
DEV unsigned ld_flag(unsigned* p, unsigned tag) { const uint64_t x = __hip_atomic_load((uint64_t*)p, __ATOMIC_RELAXED, __HIP_MEMORY_SCOPE_AGENT); return (unsigned)(x >> 32) == tag ? (unsigned)x : 0u; }
DEV void vm_wait0() { asm volatile("s_waitcnt vmcnt(0)" ::: "memory"); }
DEV void raw_barrier() { asm volatile("s_waitcnt lgkmcnt(0)" ::: "memory"); __builtin_amdgcn_s_barrier(); asm volatile("" ::: "memory"); }
DEV unsigned pack2(float a, float b) { f32x2 x = {a, b}; return __builtin_bit_cast(unsigned, __builtin_convertvector(x, bf16x2)); }
DEV bf16x4 tr_read(ldsp p) { return __builtin_amdgcn_ds_read_tr16_b64_v4bf16((LAS bf16x4*)p); }
DEV bf16x8 cat8(bf16x4 a, bf16x4 b) { return __builtin_shufflevector(a, b, 0, 1, 2, 3, 4, 5, 6, 7); }
DEV bf16x16 cat16(bf16x8 a, bf16x8 b) { return __builtin_shufflevector(a, b, 0, 1, 2, 3, 4, 5, 6, 7, 8, 9, 10, 11, 12, 13, 14, 15); }
DEV f32x4 mfma16(bf16x8 a, bf16x8 b, f32x4 c) { return __builtin_amdgcn_mfma_f32_16x16x32_bf16(a, b, c, 0, 0, 0); }
DEV f32x4 mfma16k16(bf16x4 a, bf16x4 b, f32x4 c) {
  const bf16x4 z = {(bf16)0.f, (bf16)0.f, (bf16)0.f, (bf16)0.f};
  return __builtin_amdgcn_mfma_f32_16x16x32_bf16(cat8(a, z), cat8(b, z), c, 0, 0, 0); }
DEV f32x4 smf(bf16x8 a, bf16x16 b, f32x4 c, int idx) { return __builtin_amdgcn_smfmac_f32_16x16x64_bf16(a, b, c, idx, 0, 0); }
DEV f32x16 mfma32(bf16x8 a, bf16x8 b, f32x16 c) { return __builtin_amdgcn_mfma_f32_32x32x16_bf16(a, b, c, 0, 0, 0); }
DEV float fbits(unsigned u) { return __builtin_bit_cast(float, u); }
DEV unsigned ubits(float f) { return __builtin_bit_cast(unsigned, f); }
DEV float half_sum2(float a, float b) { auto r = __builtin_amdgcn_permlane32_swap(ubits(a), ubits(b), false, false); return fbits(r[0]) + fbits(r[1]); }
DEV __amdgpu_buffer_rsrc_t mk_rsrc(const void* p, unsigned bytes) { return __builtin_amdgcn_make_buffer_rsrc((void*)p, 0, bytes, 0x00020000); }
constexpr int AUX_SC1 = 16;
DEV u32x4 bld16(__amdgpu_buffer_rsrc_t r, unsigned v, unsigned s) { return __builtin_amdgcn_raw_buffer_load_b128(r, v, s, AUX_SC1); }
DEV void bst16(u32x4 d, __amdgpu_buffer_rsrc_t r, unsigned v, unsigned s) { __builtin_amdgcn_raw_buffer_store_b128(d, r, v, s, AUX_SC1); }
DEV void bst8(u32x2 d, __amdgpu_buffer_rsrc_t r, unsigned v, unsigned s) { __builtin_amdgcn_raw_buffer_store_b64(d, r, v, s, AUX_SC1); }
DEV unsigned umax3(unsigned a, unsigned b, unsigned c) { return max(max(a, b), c); }
DEV unsigned umax4(u32x4 a) { return max(umax3(a.x, a.y, a.z), a.w); }
constexpr unsigned SENT = 0xFFFFFFFFu;
DEV unsigned nosent(unsigned x) { return min(x, 0xFFFFFFFEu); }

struct Args {
  const bf16 *x, *mem0, *Wq, *Wk, *Wv, *Wo, *w1, *w2, *gw;
  bf16* out;
  unsigned char* ws;
  unsigned* ctrl;
  unsigned tag, pad0;
  int d1, d2;
};

constexpr int L_MEM = 0, L_QT = 8192, L_KT = 10240, L_OI = 12288, L_VT = 14336, L_MISC = 14336, L_KX = 16384, L_VX = 24576, L_ST = 32768;
constexpr int L_WV = L_ST + 4 * 16384;
constexpr int LDS_BYTES = 160 * 1024;
constexpr unsigned H2_SLOT = 8 * 4096, TS_OFF = 4 * 4096;
constexpr unsigned H1_PAGE = 32;
constexpr unsigned REGION = 2u << 20;
DEV unsigned page_off(unsigned q) { return (((q >> 1) << 2) + (q & 1)) << 12; }
constexpr int SPIN_MAX = 1 << 18;

DEV unsigned swz_f(unsigned row) { return (row & 3) | (((row >> 3) & 1) << 2); }
DEV unsigned swz_v(unsigned row) { return ((row >> 1) & 1) | (((row >> 3) & 1) << 1); }
DEV unsigned off_b(unsigned row, unsigned ch) { return 256u * row + 16u * (ch ^ (((row & 3) << 2) | ((row >> 2) & 3))); }

template <int N> DEV void vm_wait() { asm volatile("s_waitcnt vmcnt(%0)" :: "n"(N) : "memory"); }

#define PIN1(x) asm volatile("" : "+v"(x))
#define LP(off) ((ldsp)(size_t)(off))
typedef unsigned u32x8 __attribute__((ext_vector_type(8)));
typedef unsigned u32x16 __attribute__((ext_vector_type(16)));
typedef unsigned u32x32 __attribute__((ext_vector_type(32)));
DEV u32x16 catw(bf16x16 a, bf16x16 b) { return __builtin_shufflevector(__builtin_bit_cast(u32x8, a), __builtin_bit_cast(u32x8, b), 0, 1, 2, 3, 4, 5, 6, 7, 8, 9, 10, 11, 12, 13, 14, 15); }
DEV u32x32 catw(u32x16 a, u32x16 b) { return __builtin_shufflevector(a, b, 0, 1, 2, 3, 4, 5, 6, 7, 8, 9, 10, 11, 12, 13, 14, 15, 16, 17, 18, 19, 20, 21, 22, 23, 24, 25, 26, 27, 28, 29, 30, 31); }
DEV unsigned sgpr(unsigned x) { return __builtin_amdgcn_readfirstlane(x); }
DEV u32x4 rsrc_words(const void* p, unsigned bytes) { const uint64_t a = (uint64_t)p; return u32x4{sgpr((unsigned)a), sgpr((unsigned)(a >> 32) & 0xFFFFu), bytes, 0x00020000u}; }
DEV void glds(const void* base, unsigned voff, unsigned dst) {
  unsigned keep;
  asm volatile("s_mov_b32 %0, m0\n\ts_mov_b32 m0, %3\n\ts_nop 0\n\tglobal_load_lds_dwordx4 %1, %2\n\ts_mov_b32 m0, %0" : "=&s"(keep) : "v"(voff), "s"(base), "s"(dst) : "memory");
}
#define CHAIN_A_ASM \
  "s_mov_b32 s64, 0\n\t" \
  "s_mov_b32 s70, 0\n\t" \
  "s_mov_b32 s72, 0x3e0293ee\n\t" \
  "s_mov_b32 s65, s52\n\t" \
  "s_mov_b32 s73, s53\n\t" \
  "s_mov_b32 s66, s54\n\t" \
  "s_lshl_b32 s11, s99, 12\n\t" \
  "s_xor_b32 s11, s11, s55\n\t" \
  "v_mov_b64 v[12:13], 0\n\t" \
  "v_mov_b64 v[14:15], 0\n\t" \
  "v_mov_b64 v[16:17], 0\n\t" \
  "v_mov_b64 v[18:19], 0\n\t" \
  "v_mov_b32 v24, 0x3f803f80\n\t" \
  "v_mov_b32 v25, 0x3f803f80\n\t" \
  "v_mov_b32 v26, 0x3f803f80\n\t" \
  "v_mov_b32 v27, 0x3f803f80\n\t" \
  "s_waitcnt lgkmcnt(0)\n\t" \
  "s_barrier\n\t" \
  "ds_read_b128 v[64:67], v6 offset:0\n\t" \
  "ds_read_b128 v[68:71], v6 offset:1024\n\t" \
  "ds_read_b128 v[72:75], v6 offset:2048\n\t" \
  "ds_read_b128 v[76:79], v6 offset:3072\n\t" \
  "ds_read_b128 v[80:83], v6 offset:4096\n\t" \
  "ds_read_b128 v[84:87], v6 offset:5120\n\t" \
  "ds_read_b128 v[88:91], v6 offset:6144\n\t" \
  "ds_read_b128 v[92:95], v6 offset:7168\n\t" \
  "ds_read_b64_tr_b16 v[20:21], v7 offset:0\n\t" \
  "ds_read_b64_tr_b16 v[22:23], v7 offset:512\n\t" \
  "s_setprio 3\n\t" \
  ".p2align 6\n\t" \
  "cha_loop:\n\t" \
  "s_waitcnt lgkmcnt(0)\n\t" \
  "s_barrier\n\t" \
  "ds_read_b128 v[32:35], v1 offset:0\n\t" \
  "ds_read_b128 v[36:39], v1 offset:1024\n\t" \
  "ds_read_b128 v[40:43], v1 offset:2048\n\t" \
  "ds_read_b128 v[44:47], v1 offset:3072\n\t" \
  "ds_read_b128 v[48:51], v1 offset:4096\n\t" \
  "ds_read_b128 v[52:55], v1 offset:5120\n\t" \
  "ds_read_b128 v[56:59], v1 offset:6144\n\t" \
  "ds_read_b128 v[60:63], v1 offset:7168\n\t" \
  "s_waitcnt lgkmcnt(7)\n\t" \
  "v_smfmac_f32_16x16x64_bf16 v[12:15], v[32:35], a[0:7], v0\n\t" \
  "v_smfmac_f32_16x16x64_bf16 v[16:19], v[32:35], a[8:15], v0\n\t" \
  "s_waitcnt lgkmcnt(6)\n\t" \
  "v_smfmac_f32_16x16x64_bf16 v[12:15], v[36:39], a[16:23], v0\n\t" \
  "v_smfmac_f32_16x16x64_bf16 v[16:19], v[36:39], a[24:31], v0\n\t" \
  "s_waitcnt lgkmcnt(5)\n\t" \
  "v_smfmac_f32_16x16x64_bf16 v[12:15], v[40:43], a[32:39], v0\n\t" \
  "v_smfmac_f32_16x16x64_bf16 v[16:19], v[40:43], a[40:47], v0\n\t" \
  "s_waitcnt lgkmcnt(4)\n\t" \
  "v_smfmac_f32_16x16x64_bf16 v[12:15], v[44:47], a[48:55], v0\n\t" \
  "v_smfmac_f32_16x16x64_bf16 v[16:19], v[44:47], a[56:63], v0\n\t" \
  "s_waitcnt lgkmcnt(3)\n\t" \
  "v_smfmac_f32_16x16x64_bf16 v[12:15], v[48:51], a[64:71], v0\n\t" \
  "v_smfmac_f32_16x16x64_bf16 v[16:19], v[48:51], a[72:79], v0\n\t" \
  "s_waitcnt lgkmcnt(2)\n\t" \
  "v_smfmac_f32_16x16x64_bf16 v[12:15], v[52:55], a[80:87], v0\n\t" \
  "v_smfmac_f32_16x16x64_bf16 v[16:19], v[52:55], a[88:95], v0\n\t" \
  "s_waitcnt lgkmcnt(1)\n\t" \
  "v_smfmac_f32_16x16x64_bf16 v[12:15], v[56:59], a[96:103], v0\n\t" \
  "v_smfmac_f32_16x16x64_bf16 v[16:19], v[56:59], a[104:111], v0\n\t" \
  "s_waitcnt lgkmcnt(0)\n\t" \
  "v_smfmac_f32_16x16x64_bf16 v[12:15], v[60:63], a[112:119], v0\n\t" \
  "v_smfmac_f32_16x16x64_bf16 v[16:19], v[60:63], a[120:127], v0\n\t" \
  "s_nop 6\n\t" \
  "v_add_f32 v12, v12, v14\n\t" \
  "v_add_f32 v13, v13, v15\n\t" \
  "v_add_f32 v16, v16, v18\n\t" \
  "v_add_f32 v17, v17, v19\n\t" \
  "v_cvt_pk_bf16_f32 v12, v12, v13\n\t" \
  "v_cvt_pk_bf16_f32 v16, v16, v17\n\t" \
  "ds_write2st64_b32 v2, v12, v16 offset1:1\n\t" \
  "s_setprio 0\n\t" \
  "s_waitcnt lgkmcnt(0)\n\t" \
  "s_barrier\n\t" \
  "ds_read_b64_tr_b16 v[32:33], v5 offset:8192\n\t" \
  "ds_read_b64_tr_b16 v[34:35], v5 offset:8256\n\t" \
  "ds_read_b64_tr_b16 v[36:37], v5 offset:8704\n\t" \
  "ds_read_b64_tr_b16 v[38:39], v5 offset:8768\n\t" \
  "ds_read_b64_tr_b16 v[40:41], v5 offset:9216\n\t" \
  "ds_read_b64_tr_b16 v[42:43], v5 offset:9280\n\t" \
  "ds_read_b64_tr_b16 v[44:45], v5 offset:9728\n\t" \
  "ds_read_b64_tr_b16 v[46:47], v5 offset:9792\n\t" \
  "s_waitcnt lgkmcnt(6)\n\t" \
  "v_mfma_f32_16x16x32_bf16 v[12:15], v[64:67], v[32:35], 0\n\t" \
  "v_mfma_f32_16x16x32_bf16 v[16:19], v[68:71], v[32:35], 0\n\t" \
  "s_waitcnt lgkmcnt(4)\n\t" \
  "v_mfma_f32_16x16x32_bf16 v[12:15], v[72:75], v[36:39], v[12:15]\n\t" \
  "v_mfma_f32_16x16x32_bf16 v[16:19], v[76:79], v[36:39], v[16:19]\n\t" \
  "s_waitcnt lgkmcnt(2)\n\t" \
  "v_mfma_f32_16x16x32_bf16 v[12:15], v[80:83], v[40:43], v[12:15]\n\t" \
  "v_mfma_f32_16x16x32_bf16 v[16:19], v[84:87], v[40:43], v[16:19]\n\t" \
  "s_waitcnt lgkmcnt(0)\n\t" \
  "s_barrier\n\t" \
  "s_setprio 3\n\t" \
  "ds_read_b64_tr_b16 v[48:49], v5 offset:10240\n\t" \
  "ds_read_b64_tr_b16 v[50:51], v5 offset:10304\n\t" \
  "ds_read_b64_tr_b16 v[52:53], v5 offset:10752\n\t" \
  "ds_read_b64_tr_b16 v[54:55], v5 offset:10816\n\t" \
  "ds_read_b64_tr_b16 v[56:57], v5 offset:11264\n\t" \
  "ds_read_b64_tr_b16 v[58:59], v5 offset:11328\n\t" \
  "ds_read_b64_tr_b16 v[60:61], v5 offset:11776\n\t" \
  "ds_read_b64_tr_b16 v[62:63], v5 offset:11840\n\t" \
  "v_mfma_f32_16x16x32_bf16 v[12:15], v[88:91], v[44:47], v[12:15]\n\t" \
  "v_mfma_f32_16x16x32_bf16 v[16:19], v[92:95], v[44:47], v[16:19]\n\t" \
  "v_mov_b64 v[74:75], 0\n\t" \
  "v_mov_b64 v[78:79], 0\n\t" \
  "s_waitcnt lgkmcnt(6)\n\t" \
  "v_mfma_f32_16x16x32_bf16 v[64:67], v[48:51], v[32:35], 0\n\t" \
  "s_waitcnt lgkmcnt(4)\n\t" \
  "v_mfma_f32_16x16x32_bf16 v[64:67], v[52:55], v[36:39], v[64:67]\n\t" \
  "s_waitcnt lgkmcnt(2)\n\t" \
  "v_mfma_f32_16x16x32_bf16 v[64:67], v[56:59], v[40:43], v[64:67]\n\t" \
  "v_max3_f32 v11, v12, v13, v14\n\t" \
  "s_waitcnt lgkmcnt(0)\n\t" \
  "v_mfma_f32_16x16x32_bf16 v[64:67], v[60:63], v[44:47], v[64:67]\n\t" \
  "v_max3_f32 v28, v17, v18, v19\n\t" \
  "v_max3_f32 v11, v11, v15, v16\n\t" \
  "v_max_f32 v11, v11, v28\n\t" \
  "v_mov_b32 v28, v11\n\t" \
  "v_mov_b64 v[80:81], 0\n\t" \
  "v_mov_b64 v[82:83], 0\n\t" \
  "v_permlane32_swap_b32 v11, v28\n\t" \
  "v_max_f32 v11, v11, v28\n\t" \
  "v_max3_f32 v11, v11, v64, v65\n\t" \
  "v_max3_f32 v11, v11, v66, v67\n\t" \
  "v_mov_b32 v28, v11\n\t" \
  "v_mov_b64 v[84:85], 0\n\t" \
  "v_mov_b64 v[86:87], 0\n\t" \
  "v_permlane16_swap_b32 v11, v28\n\t" \
  "v_max_f32 v11, v11, v28\n\t" \
  "v_mul_f32 v11, s72, v11\n\t" \
  "v_fma_f32 v68, v12, s72, -v11\n\t" \
  "v_fma_f32 v28, v13, s72, -v11\n\t" \
  "v_fma_f32 v69, v14, s72, -v11\n\t" \
  "v_fma_f32 v29, v15, s72, -v11\n\t" \
  "v_exp_f32 v68, v68\n\t" \
  "v_exp_f32 v28, v28\n\t" \
  "v_exp_f32 v69, v69\n\t" \
  "v_exp_f32 v29, v29\n\t" \
  "v_cvt_pk_bf16_f32 v68, v68, v28\n\t" \
  "v_cvt_pk_bf16_f32 v69, v69, v29\n\t" \
  "v_fma_f32 v70, v16, s72, -v11\n\t" \
  "v_fma_f32 v28, v17, s72, -v11\n\t" \
  "v_fma_f32 v71, v18, s72, -v11\n\t" \
  "v_fma_f32 v29, v19, s72, -v11\n\t" \
  "v_exp_f32 v70, v70\n\t" \
  "v_exp_f32 v28, v28\n\t" \
  "v_exp_f32 v71, v71\n\t" \
  "v_exp_f32 v29, v29\n\t" \
  "v_cvt_pk_bf16_f32 v70, v70, v28\n\t" \
  "v_cvt_pk_bf16_f32 v71, v71, v29\n\t" \
  "v_add_f32 v11, v11, v10\n\t" \
  "s_nop 0\n\t" \
  "v_mfma_f32_16x16x32_bf16 v[48:51], v[24:27], v[68:71], 0\n\t" \
  "v_mfma_f32_16x16x32_bf16 v[52:55], v[20:23], v[68:71], 0\n\t" \
  "s_barrier\n\t" \
  "ds_read_b64 v[76:77], v9\n\t" \
  "v_fma_f32 v72, v64, s72, -v11\n\t" \
  "v_fma_f32 v28, v65, s72, -v11\n\t" \
  "v_fma_f32 v73, v66, s72, -v11\n\t" \
  "v_fma_f32 v29, v67, s72, -v11\n\t" \
  "v_exp_f32 v72, v72\n\t" \
  "v_exp_f32 v28, v28\n\t" \
  "v_exp_f32 v73, v73\n\t" \
  "v_exp_f32 v29, v29\n\t" \
  "v_cvt_pk_bf16_f32 v72, v72, v28\n\t" \
  "v_cvt_pk_bf16_f32 v73, v73, v29\n\t" \
  "s_waitcnt lgkmcnt(0)\n\t" \
  "s_nop 0\n\t" \
  "v_mfma_f32_16x16x32_bf16 v[48:51], v[24:27], v[72:75], v[48:51]\n\t" \
  "v_mfma_f32_16x16x32_bf16 v[52:55], v[76:79], v[72:75], v[52:55]\n\t" \
  "v_mov_b64 v[88:89], 0\n\t" \
  "v_mov_b64 v[90:91], 0\n\t" \
  "v_mov_b64 v[92:93], 0\n\t" \
  "v_mov_b64 v[94:95], 0\n\t" \
  "s_nop 1\n\t" \
  "s_waitcnt vmcnt(0)\n\t" \
  "v_rcp_f32 v11, v48\n\t" \
  "s_nop 0\n\t" \
  "v_mul_f32 v52, v11, v52\n\t" \
  "v_mul_f32 v53, v11, v53\n\t" \
  "v_mul_f32 v54, v11, v54\n\t" \
  "v_mul_f32 v55, v11, v55\n\t" \
  "v_cvt_pk_bf16_f32 v28, v52, v53\n\t" \
  "v_cvt_pk_bf16_f32 v29, v54, v55\n\t" \
  "ds_write_b64 v8, v[28:29]\n\t" \
  "s_waitcnt lgkmcnt(0)\n\t" \
  "s_barrier\n\t" \
  "ds_read_b128 v[56:59], v1 offset:12288\n\t" \
  "s_waitcnt lgkmcnt(0)\n\t" \
  "v_smfmac_f32_16x16x64_bf16 v[80:83], v[56:59], v[96:103], v0\n\t" \
  "v_smfmac_f32_16x16x64_bf16 v[84:87], v[56:59], v[104:111], v0\n\t" \
  "v_smfmac_f32_16x16x64_bf16 v[88:91], v[56:59], v[112:119], v0\n\t" \
  "v_smfmac_f32_16x16x64_bf16 v[92:95], v[56:59], v[120:127], v0\n\t" \
  "s_nop 4\n\t" \
  "v_add_f32 v80, v80, v82\n\t" \
  "v_add_f32 v81, v81, v83\n\t" \
  "v_add_f32 v84, v84, v86\n\t" \
  "v_add_f32 v85, v85, v87\n\t" \
  "v_cvt_pk_bf16_f32 v60, v80, v84\n\t" \
  "v_cvt_pk_bf16_f32 v62, v81, v85\n\t" \
  "v_add_f32 v88, v88, v90\n\t" \
  "v_add_f32 v89, v89, v91\n\t" \
  "v_add_f32 v92, v92, v94\n\t" \
  "v_add_f32 v93, v93, v95\n\t" \
  "v_cvt_pk_bf16_f32 v61, v88, v92\n\t" \
  "v_cvt_pk_bf16_f32 v63, v89, v93\n\t" \
  "buffer_store_dwordx4 v[60:63], v4, s[36:39], s65 offen sc1\n\t" \
  "v_mov_b64 v[12:13], 0\n\t" \
  "v_mov_b64 v[14:15], 0\n\t" \
  "v_mov_b64 v[16:17], 0\n\t" \
  "v_mov_b64 v[18:19], 0\n\t" \
  "s_barrier\n\t" \
  "v_mov_b32 v60, s29\n\t" \
  "ds_read_b32 v60, v60 offset:13312\n\t" \
  "s_and_b32 s67, s64, 3\n\t" \
  "s_lshl_b32 s67, s67, 15\n\t" \
  "s_add_u32 s67, s67, s52\n\t" \
  "s_add_u32 s25, s67, s55\n\t" \
  "s_waitcnt lgkmcnt(0)\n\t" \
  "v_readfirstlane_b32 s26, v60\n\t" \
  "cha_w2:\n\t" \
  "s_memrealtime s[4:5]\n\t" \
  "s_waitcnt lgkmcnt(0)\n\t" \
  "s_sub_u32 s18, s4, s26\n\t" \
  "s_cmp_lt_i32 s18, 0\n\t" \
  "s_cbranch_scc1 cha_w2\n\t" \
  "buffer_load_dwordx4 v[32:35], v3, s[36:39], s25 offen sc1\n\t" \
  "ds_read_b128 v[64:67], v6 offset:0\n\t" \
  "ds_read_b128 v[68:71], v6 offset:1024\n\t" \
  "ds_read_b128 v[72:75], v6 offset:2048\n\t" \
  "ds_read_b128 v[76:79], v6 offset:3072\n\t" \
  "ds_read_b128 v[80:83], v6 offset:4096\n\t" \
  "ds_read_b128 v[84:87], v6 offset:5120\n\t" \
  "ds_read_b128 v[88:91], v6 offset:6144\n\t" \
  "ds_read_b128 v[92:95], v6 offset:7168\n\t" \
  "ds_read_b64_tr_b16 v[20:21], v7 offset:0\n\t" \
  "ds_read_b64_tr_b16 v[22:23], v7 offset:512\n\t" \
  "v_mov_b64 v[36:37], -1\n\t" \
  "v_mov_b64 v[38:39], -1\n\t" \
  "buffer_store_dwordx4 v[36:39], v4, s[36:39], s66 offen sc1\n\t" \
  "v_add_u32 v61, s11, v1\n\t" \
  "s_waitcnt vmcnt(1)\n\t" \
  "v_max_u32 v60, v32, v33\n\t" \
  "v_max3_u32 v60, v60, v34, v35\n\t" \
  "v_cmp_eq_u32 vcc, -1, v60\n\t" \
  "s_cbranch_vccnz cha_p2s\n\t" \
  "cha_p2e:\n\t" \
  "ds_write_b128 v61, v[32:35] offset:0\n\t" \
  "s_mov_b32 s18, s66\n\t" \
  "s_mov_b32 s66, s65\n\t" \
  "s_mov_b32 s65, s73\n\t" \
  "s_mov_b32 s73, s18\n\t" \
  "s_add_u32 s64, s64, 1\n\t" \
  "s_cmp_lt_u32 s64, 63\n\t" \
  "s_cbranch_scc1 cha_loop\n\t" \
  "s_waitcnt lgkmcnt(0)\n\t" \
  "s_barrier\n\t" \
  "s_endpgm\n\t" \
  "cha_p2s:\n\t" \
  "s_cmp_lg_u32 s71, 0\n\t" \
  "s_cbranch_scc1 cha_p2e\n\t" \
  "cha_p2r:\n\t" \
  "s_mov_b64 exec, vcc\n\t" \
  "buffer_load_dwordx4 v[32:35], v3, s[36:39], s25 offen sc1\n\t" \
  "s_add_u32 s70, s70, 1\n\t" \
  "s_waitcnt vmcnt(0)\n\t" \
  "v_max_u32 v60, v32, v33\n\t" \
  "v_max3_u32 v60, v60, v34, v35\n\t" \
  "v_cmp_eq_u32 vcc, -1, v60\n\t" \
  "s_cbranch_vccz cha_p2d\n\t" \
  "s_cmp_lt_u32 s70, 200000\n\t" \
  "s_cbranch_scc1 cha_p2r\n\t" \
  "s_mov_b32 s71, 1\n\t" \
  "v_mov_b32 v60, 0\n\t" \
  "v_mov_b32 v32, 1\n\t" \
  "v_mov_b32 v33, s96\n\t" \
  "global_store_dwordx2 v60, v[32:33], s[62:63] sc1\n\t" \
  "cha_p2d:\n\t" \
  "s_mov_b64 exec, -1\n\t" \
  "s_branch cha_p2e\n\t" \
  ""
#define CHAIN_B_ASM \
  "s_mov_b32 s64, 0\n\t" \
  "s_mov_b32 s70, 0\n\t" \
  "s_mov_b32 s72, 0x3e0293ee\n\t" \
  "s_mov_b32 s65, s52\n\t" \
  "s_mov_b32 s73, s53\n\t" \
  "s_mov_b32 s66, s54\n\t" \
  "s_lshl_b32 s11, s99, 12\n\t" \
  "s_xor_b32 s11, s11, s55\n\t" \
  "v_mov_b64 v[12:13], 0\n\t" \
  "v_mov_b64 v[14:15], 0\n\t" \
  "v_mov_b64 v[16:17], 0\n\t" \
  "v_mov_b64 v[18:19], 0\n\t" \
  "s_add_u32 s12, s29, 10240\n\t" \
  "s_add_u32 s13, s29, 14336\n\t" \
  "s_mov_b32 s27, 0\n\t" \
  "s_mov_b32 s3, 1\n\t" \
  "v_mov_b64 v[10:11], 0\n\t" \
  "s_waitcnt lgkmcnt(0)\n\t" \
  "s_barrier\n\t" \
  "ds_read_b128 v[64:67], v5 offset:0\n\t" \
  "ds_read_b128 v[68:71], v5 offset:1024\n\t" \
  "ds_read_b128 v[72:75], v5 offset:8192\n\t" \
  "ds_read_b128 v[76:79], v5 offset:9216\n\t" \
  "ds_read_b128 v[24:27], v5 offset:16384\n\t" \
  "ds_read_b128 v[28:31], v5 offset:17408\n\t" \
  "v_mov_b64 v[20:21], 0\n\t" \
  "v_mov_b64 v[22:23], 0\n\t" \
  "s_setprio 1\n\t" \
  ".p2align 6\n\t" \
  "chb_loop:\n\t" \
  "s_waitcnt lgkmcnt(0)\n\t" \
  "s_barrier\n\t" \
  "ds_read_b128 v[32:35], v1 offset:0\n\t" \
  "ds_read_b128 v[36:39], v1 offset:1024\n\t" \
  "ds_read_b128 v[40:43], v1 offset:2048\n\t" \
  "ds_read_b128 v[44:47], v1 offset:3072\n\t" \
  "ds_read_b128 v[48:51], v1 offset:4096\n\t" \
  "ds_read_b128 v[52:55], v1 offset:5120\n\t" \
  "ds_read_b128 v[56:59], v1 offset:6144\n\t" \
  "ds_read_b128 v[60:63], v1 offset:7168\n\t" \
  "s_waitcnt lgkmcnt(7)\n\t" \
  "v_smfmac_f32_16x16x64_bf16 v[12:15], v[32:35], a[0:7], v0\n\t" \
  "v_smfmac_f32_16x16x64_bf16 v[16:19], v[32:35], a[8:15], v0\n\t" \
  "s_waitcnt lgkmcnt(6)\n\t" \
  "v_smfmac_f32_16x16x64_bf16 v[12:15], v[36:39], a[16:23], v0\n\t" \
  "v_smfmac_f32_16x16x64_bf16 v[16:19], v[36:39], a[24:31], v0\n\t" \
  "s_waitcnt lgkmcnt(5)\n\t" \
  "v_smfmac_f32_16x16x64_bf16 v[12:15], v[40:43], a[32:39], v0\n\t" \
  "v_smfmac_f32_16x16x64_bf16 v[16:19], v[40:43], a[40:47], v0\n\t" \
  "s_waitcnt lgkmcnt(4)\n\t" \
  "v_smfmac_f32_16x16x64_bf16 v[12:15], v[44:47], a[48:55], v0\n\t" \
  "v_smfmac_f32_16x16x64_bf16 v[16:19], v[44:47], a[56:63], v0\n\t" \
  "s_waitcnt lgkmcnt(3)\n\t" \
  "v_smfmac_f32_16x16x64_bf16 v[12:15], v[48:51], a[64:71], v0\n\t" \
  "v_smfmac_f32_16x16x64_bf16 v[16:19], v[48:51], a[72:79], v0\n\t" \
  "s_waitcnt lgkmcnt(2)\n\t" \
  "v_smfmac_f32_16x16x64_bf16 v[12:15], v[52:55], a[80:87], v0\n\t" \
  "v_smfmac_f32_16x16x64_bf16 v[16:19], v[52:55], a[88:95], v0\n\t" \
  "s_waitcnt lgkmcnt(1)\n\t" \
  "v_smfmac_f32_16x16x64_bf16 v[12:15], v[56:59], a[96:103], v0\n\t" \
  "v_smfmac_f32_16x16x64_bf16 v[16:19], v[56:59], a[104:111], v0\n\t" \
  "s_barrier\n\t" \
  "s_waitcnt lgkmcnt(0)\n\t" \
  "v_smfmac_f32_16x16x64_bf16 v[12:15], v[60:63], a[112:119], v0\n\t" \
  "v_smfmac_f32_16x16x64_bf16 v[16:19], v[60:63], a[120:127], v0\n\t" \
  "s_nop 6\n\t" \
  "v_add_f32 v12, v12, v14\n\t" \
  "v_add_f32 v13, v13, v15\n\t" \
  "v_add_f32 v16, v16, v18\n\t" \
  "v_add_f32 v17, v17, v19\n\t" \
  "v_cvt_pk_bf16_f32 v12, v12, v13\n\t" \
  "v_cvt_pk_bf16_f32 v16, v16, v17\n\t" \
  "ds_write2st64_b32 v2, v12, v16 offset1:1\n\t" \
  "ds_read_b128 v[12:15], v5 offset:24576\n\t" \
  "ds_read_b128 v[16:19], v5 offset:25600\n\t" \
  "v_smfmac_f32_16x16x64_bf16 v[20:23], v[32:35], v[64:71], v0\n\t" \
  "ds_read_b128 v[64:67], v5 offset:32768\n\t" \
  "ds_read_b128 v[68:71], v5 offset:33792\n\t" \
  "v_smfmac_f32_16x16x64_bf16 v[20:23], v[36:39], v[72:79], v0\n\t" \
  "ds_read_b128 v[72:75], v5 offset:40960\n\t" \
  "ds_read_b128 v[76:79], v5 offset:41984\n\t" \
  "ds_read_b128 v[32:35], v5 offset:49152\n\t" \
  "ds_read_b128 v[36:39], v5 offset:50176\n\t" \
  "v_smfmac_f32_16x16x64_bf16 v[20:23], v[40:43], v[24:31], v0\n\t" \
  "ds_read_b128 v[24:27], v5 offset:57344\n\t" \
  "ds_read_b128 v[28:31], v5 offset:58368\n\t" \
  "s_waitcnt lgkmcnt(10)\n\t" \
  "s_barrier\n\t" \
  "s_waitcnt lgkmcnt(8)\n\t" \
  "v_smfmac_f32_16x16x64_bf16 v[20:23], v[44:47], v[12:19], v0\n\t" \
  "s_waitcnt lgkmcnt(6)\n\t" \
  "v_smfmac_f32_16x16x64_bf16 v[20:23], v[48:51], v[64:71], v0\n\t" \
  "s_waitcnt lgkmcnt(4)\n\t" \
  "v_smfmac_f32_16x16x64_bf16 v[20:23], v[52:55], v[72:79], v0\n\t" \
  "s_waitcnt lgkmcnt(2)\n\t" \
  "v_smfmac_f32_16x16x64_bf16 v[20:23], v[56:59], v[32:39], v0\n\t" \
  "s_waitcnt lgkmcnt(0)\n\t" \
  "v_smfmac_f32_16x16x64_bf16 v[20:23], v[60:63], v[24:31], v0\n\t" \
  "s_nop 7\n\t" \
  "v_add_f32 v20, v20, v22\n\t" \
  "v_add_f32 v21, v21, v23\n\t" \
  "v_cvt_pk_bf16_f32 v20, v20, v21\n\t" \
  "ds_write_b32 v6, v20\n\t" \
  "s_waitcnt lgkmcnt(0)\n\t" \
  "s_barrier\n\t" \
  "s_lshl_b32 s18, s99, 8\n\t" \
  "v_add_u32 v40, s18, v2\n\t" \
  "ds_read_b32 v40, v40\n\t" \
  "ds_read_b32 v41, v6\n\t" \
  "v_subrev_u32 v42, s12, v2\n\t" \
  "v_subrev_u32 v43, s13, v6\n\t" \
  "v_mov_b64 v[64:65], 0\n\t" \
  "v_mov_b64 v[66:67], 0\n\t" \
  "v_mov_b64 v[68:69], 0\n\t" \
  "v_mov_b64 v[70:71], 0\n\t" \
  "v_mov_b64 v[72:73], 0\n\t" \
  "v_mov_b64 v[74:75], 0\n\t" \
  "v_mov_b64 v[76:77], 0\n\t" \
  "v_mov_b64 v[78:79], 0\n\t" \
  "s_waitcnt lgkmcnt(0)\n\t" \
  "s_lshl_b32 s69, s64, 16\n\t" \
  "s_add_u32 s9, s69, s60\n\t" \
  "s_add_u32 s69, s69, s61\n\t" \
  "buffer_store_dword v40, v42, s[44:47], s9 offen sc1\n\t" \
  "buffer_store_dword v41, v43, s[48:51], s69 offen sc1\n\t" \
  "s_waitcnt vmcnt(7)\n\t" \
  "s_barrier\n\t" \
  "ds_read_b128 v[32:35], v1 offset:12288\n\t" \
  "s_waitcnt lgkmcnt(0)\n\t" \
  "v_smfmac_f32_16x16x64_bf16 v[64:67], v[32:35], v[96:103], v0\n\t" \
  "v_smfmac_f32_16x16x64_bf16 v[68:71], v[32:35], v[104:111], v0\n\t" \
  "v_smfmac_f32_16x16x64_bf16 v[72:75], v[32:35], v[112:119], v0\n\t" \
  "v_smfmac_f32_16x16x64_bf16 v[76:79], v[32:35], v[120:127], v0\n\t" \
  "s_nop 4\n\t" \
  "v_add_f32 v64, v64, v66\n\t" \
  "v_add_f32 v65, v65, v67\n\t" \
  "v_add_f32 v68, v68, v70\n\t" \
  "v_add_f32 v69, v69, v71\n\t" \
  "v_cvt_pk_bf16_f32 v36, v64, v68\n\t" \
  "v_cvt_pk_bf16_f32 v38, v65, v69\n\t" \
  "v_add_f32 v72, v72, v74\n\t" \
  "v_add_f32 v73, v73, v75\n\t" \
  "v_add_f32 v76, v76, v78\n\t" \
  "v_add_f32 v77, v77, v79\n\t" \
  "v_cvt_pk_bf16_f32 v37, v72, v76\n\t" \
  "v_cvt_pk_bf16_f32 v39, v73, v77\n\t" \
  "buffer_store_dwordx4 v[36:39], v4, s[36:39], s65 offen sc1\n\t" \
  "v_mov_b64 v[12:13], 0\n\t" \
  "v_mov_b64 v[14:15], 0\n\t" \
  "v_mov_b64 v[16:17], 0\n\t" \
  "v_mov_b64 v[18:19], 0\n\t" \
  "s_cmp_lg_u32 s3, 0\n\t" \
  "s_cbranch_scc1 chb_k0\n\t" \
  "chb_k0e:\n\t" \
  "v_xor_b32 v44, s57, v9\n\t" \
  "s_waitcnt vmcnt(1)\n\t" \
  "ds_write_b128 v9, v[80:83] offset:0\n\t" \
  "ds_write_b128 v9, v[84:87] offset:256\n\t" \
  "ds_write_b128 v44, v[88:91] offset:512\n\t" \
  "ds_write_b128 v44, v[92:95] offset:768\n\t" \
  "v_readfirstlane_b32 s27, v10\n\t" \
  "v_readfirstlane_b32 s18, v11\n\t" \
  "s_nop 3\n\t" \
  "s_cmp_eq_u32 s18, s96\n\t" \
  "s_cselect_b32 s27, s27, 0\n\t" \
  "ds_read_b128 v[64:67], v5 offset:0\n\t" \
  "ds_read_b128 v[68:71], v5 offset:1024\n\t" \
  "ds_read_b128 v[72:75], v5 offset:8192\n\t" \
  "ds_read_b128 v[76:79], v5 offset:9216\n\t" \
  "ds_read_b128 v[24:27], v5 offset:16384\n\t" \
  "ds_read_b128 v[28:31], v5 offset:17408\n\t" \
  "v_mov_b64 v[20:21], 0\n\t" \
  "v_mov_b64 v[22:23], 0\n\t" \
  "s_waitcnt lgkmcnt(0)\n\t" \
  "s_barrier\n\t" \
  "v_mov_b32 v60, s29\n\t" \
  "ds_read_b32 v60, v60 offset:13312\n\t" \
  "s_and_b32 s67, s64, 3\n\t" \
  "s_lshl_b32 s67, s67, 15\n\t" \
  "s_add_u32 s67, s67, s52\n\t" \
  "s_add_u32 s25, s67, s55\n\t" \
  "s_waitcnt lgkmcnt(0)\n\t" \
  "v_readfirstlane_b32 s26, v60\n\t" \
  "chb_w2:\n\t" \
  "s_memrealtime s[4:5]\n\t" \
  "s_waitcnt lgkmcnt(0)\n\t" \
  "s_sub_u32 s18, s4, s26\n\t" \
  "s_cmp_lt_i32 s18, 0\n\t" \
  "s_cbranch_scc1 chb_w2\n\t" \
  "buffer_load_dwordx4 v[32:35], v3, s[36:39], s25 offen sc1\n\t" \
  "s_sleep 3\n\t" \
  "v_mov_b64 v[36:37], -1\n\t" \
  "v_mov_b64 v[38:39], -1\n\t" \
  "buffer_store_dwordx4 v[36:39], v4, s[36:39], s66 offen sc1\n\t" \
  "s_add_u32 s69, s64, 3\n\t" \
  "s_min_u32 s69, s69, 64\n\t" \
  "s_cmp_lt_u32 s27, s69\n\t" \
  "s_cselect_b32 s3, 1, 0\n\t" \
  "s_add_u32 s69, s64, 2\n\t" \
  "s_lshl_b32 s69, s69, 13\n\t" \
  "s_add_u32 s69, s69, s58\n\t" \
  "s_cmp_lg_u32 s3, 0\n\t" \
  "s_cselect_b32 s69, 0x40000000, s69\n\t" \
  "buffer_load_dwordx4 v[80:83], v8, s[40:43], s69 offen sc1\n\t" \
  "s_add_u32 s69, s69, s59\n\t" \
  "buffer_load_dwordx4 v[84:87], v8, s[40:43], s69 offen sc1\n\t" \
  "s_add_u32 s69, s69, s59\n\t" \
  "buffer_load_dwordx4 v[88:91], v8, s[40:43], s69 offen sc1\n\t" \
  "s_add_u32 s69, s69, s59\n\t" \
  "buffer_load_dwordx4 v[92:95], v8, s[40:43], s69 offen sc1\n\t" \
  "v_mov_b32 v10, 0\n\t" \
  "global_load_dwordx2 v[10:11], v10, s[30:31] sc1\n\t" \
  "v_add_u32 v61, s11, v1\n\t" \
  "s_waitcnt vmcnt(6)\n\t" \
  "v_max_u32 v60, v32, v33\n\t" \
  "v_max3_u32 v60, v60, v34, v35\n\t" \
  "v_cmp_eq_u32 vcc, -1, v60\n\t" \
  "s_cbranch_vccnz chb_p2s\n\t" \
  "chb_p2e:\n\t" \
  "ds_write_b128 v61, v[32:35] offset:0\n\t" \
  "s_mov_b32 s18, s66\n\t" \
  "s_mov_b32 s66, s65\n\t" \
  "s_mov_b32 s65, s73\n\t" \
  "s_mov_b32 s73, s18\n\t" \
  "s_add_u32 s64, s64, 1\n\t" \
  "s_cmp_lt_u32 s64, 63\n\t" \
  "s_cbranch_scc1 chb_loop\n\t" \
  "s_waitcnt lgkmcnt(0)\n\t" \
  "s_barrier\n\t" \
  "ds_read_b128 v[32:35], v1 offset:0\n\t" \
  "ds_read_b128 v[36:39], v1 offset:1024\n\t" \
  "ds_read_b128 v[40:43], v1 offset:2048\n\t" \
  "ds_read_b128 v[44:47], v1 offset:3072\n\t" \
  "ds_read_b128 v[48:51], v1 offset:4096\n\t" \
  "ds_read_b128 v[52:55], v1 offset:5120\n\t" \
  "ds_read_b128 v[56:59], v1 offset:6144\n\t" \
  "ds_read_b128 v[60:63], v1 offset:7168\n\t" \
  "s_waitcnt lgkmcnt(7)\n\t" \
  "v_smfmac_f32_16x16x64_bf16 v[12:15], v[32:35], a[0:7], v0\n\t" \
  "v_smfmac_f32_16x16x64_bf16 v[16:19], v[32:35], a[8:15], v0\n\t" \
  "s_waitcnt lgkmcnt(6)\n\t" \
  "v_smfmac_f32_16x16x64_bf16 v[12:15], v[36:39], a[16:23], v0\n\t" \
  "v_smfmac_f32_16x16x64_bf16 v[16:19], v[36:39], a[24:31], v0\n\t" \
  "s_waitcnt lgkmcnt(5)\n\t" \
  "v_smfmac_f32_16x16x64_bf16 v[12:15], v[40:43], a[32:39], v0\n\t" \
  "v_smfmac_f32_16x16x64_bf16 v[16:19], v[40:43], a[40:47], v0\n\t" \
  "s_waitcnt lgkmcnt(4)\n\t" \
  "v_smfmac_f32_16x16x64_bf16 v[12:15], v[44:47], a[48:55], v0\n\t" \
  "v_smfmac_f32_16x16x64_bf16 v[16:19], v[44:47], a[56:63], v0\n\t" \
  "s_waitcnt lgkmcnt(3)\n\t" \
  "v_smfmac_f32_16x16x64_bf16 v[12:15], v[48:51], a[64:71], v0\n\t" \
  "v_smfmac_f32_16x16x64_bf16 v[16:19], v[48:51], a[72:79], v0\n\t" \
  "s_waitcnt lgkmcnt(2)\n\t" \
  "v_smfmac_f32_16x16x64_bf16 v[12:15], v[52:55], a[80:87], v0\n\t" \
  "v_smfmac_f32_16x16x64_bf16 v[16:19], v[52:55], a[88:95], v0\n\t" \
  "s_waitcnt lgkmcnt(1)\n\t" \
  "v_smfmac_f32_16x16x64_bf16 v[12:15], v[56:59], a[96:103], v0\n\t" \
  "v_smfmac_f32_16x16x64_bf16 v[16:19], v[56:59], a[104:111], v0\n\t" \
  "s_waitcnt lgkmcnt(0)\n\t" \
  "v_smfmac_f32_16x16x64_bf16 v[12:15], v[60:63], a[112:119], v0\n\t" \
  "v_smfmac_f32_16x16x64_bf16 v[16:19], v[60:63], a[120:127], v0\n\t" \
  "s_nop 6\n\t" \
  "v_add_f32 v12, v12, v14\n\t" \
  "v_add_f32 v13, v13, v15\n\t" \
  "v_add_f32 v16, v16, v18\n\t" \
  "v_add_f32 v17, v17, v19\n\t" \
  "v_cvt_pk_bf16_f32 v12, v12, v13\n\t" \
  "v_cvt_pk_bf16_f32 v16, v16, v17\n\t" \
  "ds_write2st64_b32 v2, v12, v16 offset1:1\n\t" \
  "ds_read_b128 v[12:15], v5 offset:24576\n\t" \
  "ds_read_b128 v[16:19], v5 offset:25600\n\t" \
  "v_smfmac_f32_16x16x64_bf16 v[20:23], v[32:35], v[64:71], v0\n\t" \
  "ds_read_b128 v[64:67], v5 offset:32768\n\t" \
  "ds_read_b128 v[68:71], v5 offset:33792\n\t" \
  "v_smfmac_f32_16x16x64_bf16 v[20:23], v[36:39], v[72:79], v0\n\t" \
  "ds_read_b128 v[72:75], v5 offset:40960\n\t" \
  "ds_read_b128 v[76:79], v5 offset:41984\n\t" \
  "ds_read_b128 v[32:35], v5 offset:49152\n\t" \
  "ds_read_b128 v[36:39], v5 offset:50176\n\t" \
  "v_smfmac_f32_16x16x64_bf16 v[20:23], v[40:43], v[24:31], v0\n\t" \
  "ds_read_b128 v[24:27], v5 offset:57344\n\t" \
  "ds_read_b128 v[28:31], v5 offset:58368\n\t" \
  "s_waitcnt lgkmcnt(8)\n\t" \
  "v_smfmac_f32_16x16x64_bf16 v[20:23], v[44:47], v[12:19], v0\n\t" \
  "s_waitcnt lgkmcnt(6)\n\t" \
  "v_smfmac_f32_16x16x64_bf16 v[20:23], v[48:51], v[64:71], v0\n\t" \
  "s_waitcnt lgkmcnt(4)\n\t" \
  "v_smfmac_f32_16x16x64_bf16 v[20:23], v[52:55], v[72:79], v0\n\t" \
  "s_waitcnt lgkmcnt(2)\n\t" \
  "v_smfmac_f32_16x16x64_bf16 v[20:23], v[56:59], v[32:39], v0\n\t" \
  "s_waitcnt lgkmcnt(0)\n\t" \
  "v_smfmac_f32_16x16x64_bf16 v[20:23], v[60:63], v[24:31], v0\n\t" \
  "s_nop 7\n\t" \
  "v_add_f32 v20, v20, v22\n\t" \
  "v_add_f32 v21, v21, v23\n\t" \
  "v_cvt_pk_bf16_f32 v20, v20, v21\n\t" \
  "ds_write_b32 v6, v20\n\t" \
  "s_waitcnt lgkmcnt(0)\n\t" \
  "s_lshl_b32 s18, s99, 8\n\t" \
  "v_add_u32 v40, s18, v2\n\t" \
  "ds_read_b32 v40, v40\n\t" \
  "ds_read_b32 v41, v6\n\t" \
  "v_subrev_u32 v42, s12, v2\n\t" \
  "v_subrev_u32 v43, s13, v6\n\t" \
  "s_waitcnt lgkmcnt(0)\n\t" \
  "s_lshl_b32 s69, s64, 16\n\t" \
  "s_add_u32 s9, s69, s60\n\t" \
  "s_add_u32 s69, s69, s61\n\t" \
  "buffer_store_dword v40, v42, s[44:47], s9 offen sc1\n\t" \
  "buffer_store_dword v41, v43, s[48:51], s69 offen sc1\n\t" \
  "s_waitcnt vmcnt(0)\n\t" \
  "s_barrier\n\t" \
  "v_mov_b32 v44, 0\n\t" \
  "v_mov_b32 v46, 64\n\t" \
  "v_mov_b32 v47, s96\n\t" \
  "s_mov_b64 exec, 1\n\t" \
  "global_store_dwordx2 v44, v[46:47], s[34:35] sc1\n\t" \
  "s_mov_b64 exec, -1\n\t" \
  "s_endpgm\n\t" \
  "chb_p2s:\n\t" \
  "s_cmp_lg_u32 s71, 0\n\t" \
  "s_cbranch_scc1 chb_p2e\n\t" \
  "chb_p2r:\n\t" \
  "s_mov_b64 exec, vcc\n\t" \
  "buffer_load_dwordx4 v[32:35], v3, s[36:39], s25 offen sc1\n\t" \
  "s_add_u32 s70, s70, 1\n\t" \
  "s_waitcnt vmcnt(0)\n\t" \
  "v_max_u32 v60, v32, v33\n\t" \
  "v_max3_u32 v60, v60, v34, v35\n\t" \
  "v_cmp_eq_u32 vcc, -1, v60\n\t" \
  "s_cbranch_vccz chb_p2d\n\t" \
  "s_cmp_lt_u32 s70, 200000\n\t" \
  "s_cbranch_scc1 chb_p2r\n\t" \
  "s_mov_b32 s71, 1\n\t" \
  "v_mov_b32 v60, 0\n\t" \
  "v_mov_b32 v32, 1\n\t" \
  "v_mov_b32 v33, s96\n\t" \
  "global_store_dwordx2 v60, v[32:33], s[62:63] sc1\n\t" \
  "chb_p2d:\n\t" \
  "s_mov_b64 exec, -1\n\t" \
  "s_branch chb_p2e\n\t" \
  "chb_k0:\n\t" \
  "s_waitcnt vmcnt(1)\n\t" \
  "v_readfirstlane_b32 s27, v10\n\t" \
  "v_readfirstlane_b32 s18, v11\n\t" \
  "s_nop 3\n\t" \
  "s_cmp_eq_u32 s18, s96\n\t" \
  "s_cselect_b32 s27, s27, 0\n\t" \
  "s_add_u32 s69, s64, 2\n\t" \
  "s_min_u32 s69, s69, 64\n\t" \
  "s_cmp_lt_u32 s27, s69\n\t" \
  "s_cbranch_scc1 chb_kw\n\t" \
  "chb_kwe:\n\t" \
  "s_mov_b32 s3, 0\n\t" \
  "s_add_u32 s69, s64, 1\n\t" \
  "s_lshl_b32 s69, s69, 13\n\t" \
  "s_add_u32 s69, s69, s58\n\t" \
  "buffer_load_dwordx4 v[80:83], v8, s[40:43], s69 offen sc1\n\t" \
  "s_add_u32 s69, s69, s59\n\t" \
  "buffer_load_dwordx4 v[84:87], v8, s[40:43], s69 offen sc1\n\t" \
  "s_add_u32 s69, s69, s59\n\t" \
  "buffer_load_dwordx4 v[88:91], v8, s[40:43], s69 offen sc1\n\t" \
  "s_add_u32 s69, s69, s59\n\t" \
  "buffer_load_dwordx4 v[92:95], v8, s[40:43], s69 offen sc1\n\t" \
  "v_mov_b32 v10, 0\n\t" \
  "global_load_dwordx2 v[10:11], v10, s[30:31] sc1\n\t" \
  "s_waitcnt vmcnt(0)\n\t" \
  "s_branch chb_k0e\n\t" \
  "chb_kw:\n\t" \
  "s_cmp_lg_u32 s71, 0\n\t" \
  "s_cbranch_scc1 chb_kwe\n\t" \
  "v_mov_b32 v10, 0\n\t" \
  "global_load_dwordx2 v[10:11], v10, s[30:31] sc1\n\t" \
  "s_waitcnt vmcnt(0)\n\t" \
  "v_readfirstlane_b32 s27, v10\n\t" \
  "v_readfirstlane_b32 s18, v11\n\t" \
  "s_nop 3\n\t" \
  "s_cmp_eq_u32 s18, s96\n\t" \
  "s_cselect_b32 s27, s27, 0\n\t" \
  "s_cmp_ge_u32 s27, s69\n\t" \
  "s_cbranch_scc1 chb_kwe\n\t" \
  "s_sleep 2\n\t" \
  "s_add_u32 s70, s70, 1\n\t" \
  "s_cmp_lt_u32 s70, 200000\n\t" \
  "s_cbranch_scc1 chb_kw\n\t" \
  "s_mov_b32 s71, 1\n\t" \
  "v_mov_b32 v44, 0\n\t" \
  "v_mov_b32 v10, 1\n\t" \
  "v_mov_b32 v11, s96\n\t" \
  "global_store_dwordx2 v44, v[10:11], s[62:63] sc1\n\t" \
  "s_branch chb_kwe\n\t" \
  ""
#define CHAIN_R_ASM \
  "s_mov_b32 s64, 0\n\t" \
  "s_mov_b32 s70, 0\n\t" \
  "s_mov_b32 s72, 0x3e0293ee\n\t" \
  "s_mov_b32 s65, s52\n\t" \
  "s_mov_b32 s73, s53\n\t" \
  "s_mov_b32 s66, s54\n\t" \
  "s_lshl_b32 s11, s99, 12\n\t" \
  "s_xor_b32 s11, s11, s55\n\t" \
  "v_mov_b64 v[12:13], 0\n\t" \
  "v_mov_b64 v[14:15], 0\n\t" \
  "v_mov_b64 v[16:17], 0\n\t" \
  "v_mov_b64 v[18:19], 0\n\t" \
  "s_add_u32 s12, s29, 10240\n\t" \
  "s_add_u32 s13, s29, 14336\n\t" \
  "s_mov_b32 s7, 0\n\t" \
  "s_mov_b32 s15, 0\n\t" \
  "s_mov_b32 s24, 0\n\t" \
  "s_mov_b32 s19, 0\n\t" \
  "s_mov_b32 s3, 0\n\t" \
  "v_subrev_u32 v90, s29, v1\n\t" \
  "v_lshlrev_b32 v90, 3, v90\n\t" \
  "v_mov_b32 v91, s28\n\t" \
  "s_add_u32 s14, s52, 114688\n\t" \
  "s_mov_b32 s27, 128\n\t" \
  "s_mov_b32 s2, 40\n\t" \
  "s_waitcnt lgkmcnt(0)\n\t" \
  "s_barrier\n\t" \
  "s_memrealtime s[4:5]\n\t" \
  "s_waitcnt lgkmcnt(0)\n\t" \
  "s_min_u32 s15, s4, -2\n\t" \
  "v_mov_b32 v28, s15\n\t" \
  "v_mov_b32 v29, s15\n\t" \
  "s_mov_b64 exec, 1\n\t" \
  "buffer_store_dwordx2 v[28:29], v91, s[36:39], s14 offen sc1\n\t" \
  "s_mov_b64 exec, -1\n\t" \
  "ds_read_b128 v[64:67], v5 offset:0\n\t" \
  "ds_read_b128 v[68:71], v5 offset:1024\n\t" \
  "ds_read_b128 v[72:75], v5 offset:8192\n\t" \
  "ds_read_b128 v[76:79], v5 offset:9216\n\t" \
  "ds_read_b128 v[80:83], v5 offset:16384\n\t" \
  "ds_read_b128 v[84:87], v5 offset:17408\n\t" \
  "v_mov_b64 v[20:21], 0\n\t" \
  "v_mov_b64 v[22:23], 0\n\t" \
  "s_setprio 1\n\t" \
  ".p2align 6\n\t" \
  "chr_loop:\n\t" \
  "s_waitcnt lgkmcnt(0)\n\t" \
  "s_barrier\n\t" \
  "ds_read_b128 v[32:35], v1 offset:0\n\t" \
  "ds_read_b128 v[36:39], v1 offset:1024\n\t" \
  "ds_read_b128 v[40:43], v1 offset:2048\n\t" \
  "ds_read_b128 v[44:47], v1 offset:3072\n\t" \
  "ds_read_b128 v[48:51], v1 offset:4096\n\t" \
  "ds_read_b128 v[52:55], v1 offset:5120\n\t" \
  "ds_read_b128 v[56:59], v1 offset:6144\n\t" \
  "ds_read_b128 v[60:63], v1 offset:7168\n\t" \
  "buffer_load_dwordx2 v[88:89], v90, s[36:39], s14 offen sc1\n\t" \
  "s_waitcnt lgkmcnt(7)\n\t" \
  "v_smfmac_f32_16x16x64_bf16 v[12:15], v[32:35], a[0:7], v0\n\t" \
  "v_smfmac_f32_16x16x64_bf16 v[16:19], v[32:35], a[8:15], v0\n\t" \
  "s_waitcnt lgkmcnt(6)\n\t" \
  "v_smfmac_f32_16x16x64_bf16 v[12:15], v[36:39], a[16:23], v0\n\t" \
  "v_smfmac_f32_16x16x64_bf16 v[16:19], v[36:39], a[24:31], v0\n\t" \
  "s_waitcnt lgkmcnt(5)\n\t" \
  "v_smfmac_f32_16x16x64_bf16 v[12:15], v[40:43], a[32:39], v0\n\t" \
  "v_smfmac_f32_16x16x64_bf16 v[16:19], v[40:43], a[40:47], v0\n\t" \
  "s_waitcnt lgkmcnt(4)\n\t" \
  "v_smfmac_f32_16x16x64_bf16 v[12:15], v[44:47], a[48:55], v0\n\t" \
  "v_smfmac_f32_16x16x64_bf16 v[16:19], v[44:47], a[56:63], v0\n\t" \
  "s_waitcnt lgkmcnt(3)\n\t" \
  "v_smfmac_f32_16x16x64_bf16 v[12:15], v[48:51], a[64:71], v0\n\t" \
  "v_smfmac_f32_16x16x64_bf16 v[16:19], v[48:51], a[72:79], v0\n\t" \
  "s_waitcnt lgkmcnt(2)\n\t" \
  "v_smfmac_f32_16x16x64_bf16 v[12:15], v[52:55], a[80:87], v0\n\t" \
  "v_smfmac_f32_16x16x64_bf16 v[16:19], v[52:55], a[88:95], v0\n\t" \
  "s_waitcnt lgkmcnt(1)\n\t" \
  "v_smfmac_f32_16x16x64_bf16 v[12:15], v[56:59], a[96:103], v0\n\t" \
  "v_smfmac_f32_16x16x64_bf16 v[16:19], v[56:59], a[104:111], v0\n\t" \
  "s_barrier\n\t" \
  "s_waitcnt lgkmcnt(0)\n\t" \
  "v_smfmac_f32_16x16x64_bf16 v[12:15], v[60:63], a[112:119], v0\n\t" \
  "v_smfmac_f32_16x16x64_bf16 v[16:19], v[60:63], a[120:127], v0\n\t" \
  "s_nop 6\n\t" \
  "v_add_f32 v12, v12, v14\n\t" \
  "v_add_f32 v13, v13, v15\n\t" \
  "v_add_f32 v16, v16, v18\n\t" \
  "v_add_f32 v17, v17, v19\n\t" \
  "v_cvt_pk_bf16_f32 v12, v12, v13\n\t" \
  "v_cvt_pk_bf16_f32 v16, v16, v17\n\t" \
  "ds_write2st64_b32 v2, v12, v16 offset1:1\n\t" \
  "ds_read_b128 v[12:15], v5 offset:24576\n\t" \
  "ds_read_b128 v[16:19], v5 offset:25600\n\t" \
  "v_smfmac_f32_16x16x64_bf16 v[20:23], v[32:35], v[64:71], v0\n\t" \
  "ds_read_b128 v[64:67], v5 offset:32768\n\t" \
  "ds_read_b128 v[68:71], v5 offset:33792\n\t" \
  "v_smfmac_f32_16x16x64_bf16 v[20:23], v[36:39], v[72:79], v0\n\t" \
  "ds_read_b128 v[72:75], v5 offset:40960\n\t" \
  "ds_read_b128 v[76:79], v5 offset:41984\n\t" \
  "ds_read_b128 v[32:35], v5 offset:49152\n\t" \
  "ds_read_b128 v[36:39], v5 offset:50176\n\t" \
  "v_smfmac_f32_16x16x64_bf16 v[20:23], v[40:43], v[80:87], v0\n\t" \
  "ds_read_b128 v[80:83], v5 offset:57344\n\t" \
  "ds_read_b128 v[84:87], v5 offset:58368\n\t" \
  "s_waitcnt lgkmcnt(10)\n\t" \
  "s_barrier\n\t" \
  "s_waitcnt lgkmcnt(8)\n\t" \
  "v_smfmac_f32_16x16x64_bf16 v[20:23], v[44:47], v[12:19], v0\n\t" \
  "s_waitcnt lgkmcnt(6)\n\t" \
  "v_smfmac_f32_16x16x64_bf16 v[20:23], v[48:51], v[64:71], v0\n\t" \
  "s_waitcnt lgkmcnt(4)\n\t" \
  "v_smfmac_f32_16x16x64_bf16 v[20:23], v[52:55], v[72:79], v0\n\t" \
  "s_waitcnt lgkmcnt(2)\n\t" \
  "v_smfmac_f32_16x16x64_bf16 v[20:23], v[56:59], v[32:39], v0\n\t" \
  "s_waitcnt lgkmcnt(0)\n\t" \
  "v_smfmac_f32_16x16x64_bf16 v[20:23], v[60:63], v[80:87], v0\n\t" \
  "s_nop 7\n\t" \
  "v_add_f32 v20, v20, v22\n\t" \
  "v_add_f32 v21, v21, v23\n\t" \
  "v_cvt_pk_bf16_f32 v20, v20, v21\n\t" \
  "ds_write_b32 v6, v20\n\t" \
  "s_waitcnt lgkmcnt(0)\n\t" \
  "s_barrier\n\t" \
  "s_lshl_b32 s18, s99, 8\n\t" \
  "v_add_u32 v40, s18, v2\n\t" \
  "ds_read_b32 v40, v40\n\t" \
  "ds_read_b32 v41, v6\n\t" \
  "v_subrev_u32 v42, s12, v2\n\t" \
  "v_subrev_u32 v43, s13, v6\n\t" \
  "v_mov_b64 v[64:65], 0\n\t" \
  "v_mov_b64 v[66:67], 0\n\t" \
  "v_mov_b64 v[68:69], 0\n\t" \
  "v_mov_b64 v[70:71], 0\n\t" \
  "v_mov_b64 v[72:73], 0\n\t" \
  "v_mov_b64 v[74:75], 0\n\t" \
  "v_mov_b64 v[76:77], 0\n\t" \
  "v_mov_b64 v[78:79], 0\n\t" \
  "s_waitcnt lgkmcnt(0)\n\t" \
  "s_lshl_b32 s69, s64, 16\n\t" \
  "s_add_u32 s9, s69, s60\n\t" \
  "s_add_u32 s69, s69, s61\n\t" \
  "buffer_store_dword v40, v42, s[44:47], s9 offen sc1\n\t" \
  "buffer_store_dword v41, v43, s[48:51], s69 offen sc1\n\t" \
  "s_waitcnt vmcnt(2)\n\t" \
  "chr_tc:\n\t" \
  "v_max_u32 v28, v88, v89\n\t" \
  "v_cmp_eq_u32 vcc, -1, v28\n\t" \
  "s_cbranch_vccnz chr_ts\n\t" \
  "chr_te:\n\t" \
  "v_subrev_u32 v88, s15, v88\n\t" \
  "v_subrev_u32 v89, s15, v89\n\t" \
  "s_nop 0\n\t" \
  "v_max_i32_dpp v88, v88, v88 row_shr:1\n\t" \
  "v_max_i32_dpp v89, v89, v89 row_shr:1\n\t" \
  "s_nop 0\n\t" \
  "v_max_i32_dpp v88, v88, v88 row_shr:2\n\t" \
  "v_max_i32_dpp v89, v89, v89 row_shr:2\n\t" \
  "s_nop 0\n\t" \
  "v_max_i32_dpp v88, v88, v88 row_shr:4\n\t" \
  "v_max_i32_dpp v89, v89, v89 row_shr:4\n\t" \
  "s_nop 0\n\t" \
  "v_max_i32_dpp v88, v88, v88 row_shr:8\n\t" \
  "v_max_i32_dpp v89, v89, v89 row_shr:8\n\t" \
  "s_nop 0\n\t" \
  "v_max_i32_dpp v88, v88, v88 row_bcast:15 row_mask:0xa\n\t" \
  "v_max_i32_dpp v89, v89, v89 row_bcast:15 row_mask:0xa\n\t" \
  "s_nop 0\n\t" \
  "v_max_i32_dpp v88, v88, v88 row_bcast:31 row_mask:0xc\n\t" \
  "v_max_i32_dpp v89, v89, v89 row_bcast:31 row_mask:0xc\n\t" \
  "s_nop 0\n\t" \
  "v_readlane_b32 s16, v88, 63\n\t" \
  "v_readlane_b32 s17, v89, 63\n\t" \
  "s_barrier\n\t" \
  "ds_read_b128 v[32:35], v1 offset:12288\n\t" \
  "s_waitcnt lgkmcnt(0)\n\t" \
  "v_smfmac_f32_16x16x64_bf16 v[64:67], v[32:35], v[96:103], v0\n\t" \
  "v_smfmac_f32_16x16x64_bf16 v[68:71], v[32:35], v[104:111], v0\n\t" \
  "v_smfmac_f32_16x16x64_bf16 v[72:75], v[32:35], v[112:119], v0\n\t" \
  "v_smfmac_f32_16x16x64_bf16 v[76:79], v[32:35], v[120:127], v0\n\t" \
  "s_nop 4\n\t" \
  "v_add_f32 v64, v64, v66\n\t" \
  "v_add_f32 v65, v65, v67\n\t" \
  "v_add_f32 v68, v68, v70\n\t" \
  "v_add_f32 v69, v69, v71\n\t" \
  "v_cvt_pk_bf16_f32 v36, v64, v68\n\t" \
  "v_cvt_pk_bf16_f32 v38, v65, v69\n\t" \
  "v_add_f32 v72, v72, v74\n\t" \
  "v_add_f32 v73, v73, v75\n\t" \
  "v_add_f32 v76, v76, v78\n\t" \
  "v_add_f32 v77, v77, v79\n\t" \
  "v_cvt_pk_bf16_f32 v37, v72, v76\n\t" \
  "v_cvt_pk_bf16_f32 v39, v73, v77\n\t" \
  "buffer_store_dwordx4 v[36:39], v4, s[36:39], s65 offen sc1\n\t" \
  "v_mov_b64 v[12:13], 0\n\t" \
  "v_mov_b64 v[14:15], 0\n\t" \
  "v_mov_b64 v[16:17], 0\n\t" \
  "v_mov_b64 v[18:19], 0\n\t" \
  "s_memrealtime s[4:5]\n\t" \
  "s_add_u32 s16, s16, s15\n\t" \
  "s_add_u32 s17, s17, s15\n\t" \
  "s_sub_u32 s16, s16, s7\n\t" \
  "s_sub_u32 s25, s17, s24\n\t" \
  "s_cmp_lg_u32 s3, 0\n\t" \
  "s_cselect_b32 s16, 100000, s16\n\t" \
  "s_cmp_eq_u32 s64, 0\n\t" \
  "s_cselect_b32 s25, 100000, s25\n\t" \
  "s_cselect_b32 s16, 100000, s16\n\t" \
  "s_add_u32 s27, s27, 1\n\t" \
  "s_add_u32 s2, s2, 1\n\t" \
  "s_min_u32 s27, s16, s27\n\t" \
  "s_min_u32 s2, s25, s2\n\t" \
  "s_add_u32 s10, s17, 6\n\t" \
  "s_sub_u32 s10, s10, s19\n\t" \
  "s_cmp_gt_i32 s10, 0\n\t" \
  "s_cselect_b32 s3, 1, 0\n\t" \
  "s_cmp_eq_u32 s64, 0\n\t" \
  "s_cselect_b32 s3, 1, s3\n\t" \
  "s_sub_u32 s10, s17, s19\n\t" \
  "s_add_u32 s10, s10, 60\n\t" \
  "s_max_i32 s10, s10, 45\n\t" \
  "s_add_u32 s10, s10, s19\n\t" \
  "s_add_u32 s9, s17, 0\n\t" \
  "s_cmp_eq_u32 s64, 0\n\t" \
  "s_cselect_b32 s10, s9, s10\n\t" \
  "s_cmp_lg_u32 s3, 0\n\t" \
  "s_cselect_b32 s7, s10, s19\n\t" \
  "s_sext_i32_i16 s10, s56\n\t" \
  "s_add_u32 s8, s7, s27\n\t" \
  "s_add_u32 s8, s8, s10\n\t" \
  "s_and_b32 s67, s64, 3\n\t" \
  "s_lshl_b32 s67, s67, 15\n\t" \
  "s_add_u32 s67, s67, s52\n\t" \
  "s_add_u32 s69, s65, s98\n\t" \
  "s_add_u32 s9, s69, 4096\n\t" \
  "s_waitcnt lgkmcnt(0)\n\t" \
  "s_min_u32 s6, s4, -2\n\t" \
  "s_add_u32 s10, s4, 30\n\t" \
  "s_mov_b32 s24, s8\n\t" \
  "s_ashr_i32 s18, s56, 16\n\t" \
  "s_add_u32 s19, s8, s2\n\t" \
  "s_add_u32 s19, s19, s18\n\t" \
  "s_sub_u32 s18, s8, s4\n\t" \
  "s_cmp_gt_i32 s18, 200\n\t" \
  "s_cselect_b32 s8, s10, s8\n\t" \
  "s_sub_u32 s18, s19, s4\n\t" \
  "s_add_u32 s10, s4, 70\n\t" \
  "s_cmp_gt_i32 s18, 400\n\t" \
  "s_cselect_b32 s26, s10, s19\n\t" \
  "v_mov_b32 v28, s29\n\t" \
  "v_mov_b32 v29, s26\n\t" \
  "ds_write_b32 v28, v29 offset:13312\n\t" \
  "chr_w1:\n\t" \
  "s_memrealtime s[4:5]\n\t" \
  "s_waitcnt lgkmcnt(0)\n\t" \
  "s_sub_u32 s18, s4, s8\n\t" \
  "s_cmp_lt_i32 s18, 0\n\t" \
  "s_cbranch_scc1 chr_w1\n\t" \
  "buffer_load_dwordx4 v[40:43], v8, s[36:39], s69 offen offset:0 sc1\n\t" \
  "buffer_load_dwordx4 v[44:47], v8, s[36:39], s69 offen offset:1024 sc1\n\t" \
  "buffer_load_dwordx4 v[48:51], v8, s[36:39], s69 offen offset:2048 sc1\n\t" \
  "buffer_load_dwordx4 v[52:55], v8, s[36:39], s69 offen offset:3072 sc1\n\t" \
  "buffer_load_dwordx4 v[56:59], v8, s[36:39], s9 offen offset:0 sc1\n\t" \
  "buffer_load_dwordx4 v[60:63], v8, s[36:39], s9 offen offset:1024 sc1\n\t" \
  "buffer_load_dwordx4 v[80:83], v8, s[36:39], s9 offen offset:2048 sc1\n\t" \
  "buffer_load_dwordx4 v[84:87], v8, s[36:39], s9 offen offset:3072 sc1\n\t" \
  "chr_p1m:\n\t" \
  "s_waitcnt vmcnt(7)\n\t" \
  "v_mfma_f32_16x16x32_bf16 v[20:23], v[40:43], v[24:27], 0\n\t" \
  "s_waitcnt vmcnt(6)\n\t" \
  "v_mfma_f32_16x16x32_bf16 v[20:23], v[44:47], v[24:27], v[20:23]\n\t" \
  "s_waitcnt vmcnt(5)\n\t" \
  "v_mfma_f32_16x16x32_bf16 v[20:23], v[48:51], v[24:27], v[20:23]\n\t" \
  "s_waitcnt vmcnt(4)\n\t" \
  "v_mfma_f32_16x16x32_bf16 v[20:23], v[52:55], v[24:27], v[20:23]\n\t" \
  "s_waitcnt vmcnt(3)\n\t" \
  "v_mfma_f32_16x16x32_bf16 v[20:23], v[56:59], v[24:27], v[20:23]\n\t" \
  "s_waitcnt vmcnt(2)\n\t" \
  "v_mfma_f32_16x16x32_bf16 v[20:23], v[60:63], v[24:27], v[20:23]\n\t" \
  "s_waitcnt vmcnt(1)\n\t" \
  "v_mfma_f32_16x16x32_bf16 v[20:23], v[80:83], v[24:27], v[20:23]\n\t" \
  "s_waitcnt vmcnt(0)\n\t" \
  "v_mfma_f32_16x16x32_bf16 v[20:23], v[84:87], v[24:27], v[20:23]\n\t" \
  "s_memrealtime s[16:17]\n\t" \
  "s_nop 6\n\t" \
  "v_permlane32_swap_b32 v20, v22\n\t" \
  "v_permlane32_swap_b32 v21, v23\n\t" \
  "v_add_f32 v20, v20, v22\n\t" \
  "v_add_f32 v21, v21, v23\n\t" \
  "v_cmp_u_f32 vcc, v20, v21\n\t" \
  "v_cvt_pk_bf16_f32 v20, v20, v21\n\t" \
  "s_cbranch_vccnz chr_p1s\n\t" \
  "chr_p1e:\n\t" \
  "buffer_store_dword v20, v9, s[36:39], s67 offen sc1\n\t" \
  "s_waitcnt lgkmcnt(0)\n\t" \
  "s_min_u32 s15, s16, -2\n\t" \
  "v_mov_b32 v28, s6\n\t" \
  "v_mov_b32 v29, s15\n\t" \
  "s_add_u32 s14, s67, 16384\n\t" \
  "s_mov_b64 exec, 1\n\t" \
  "buffer_store_dwordx2 v[28:29], v91, s[36:39], s14 offen sc1\n\t" \
  "s_mov_b64 exec, -1\n\t" \
  "s_barrier\n\t" \
  "s_add_u32 s25, s67, s55\n\t" \
  "ds_read_b128 v[64:67], v5 offset:0\n\t" \
  "ds_read_b128 v[68:71], v5 offset:1024\n\t" \
  "ds_read_b128 v[72:75], v5 offset:8192\n\t" \
  "ds_read_b128 v[76:79], v5 offset:9216\n\t" \
  "ds_read_b128 v[80:83], v5 offset:16384\n\t" \
  "ds_read_b128 v[84:87], v5 offset:17408\n\t" \
  "v_mov_b64 v[20:21], 0\n\t" \
  "v_mov_b64 v[22:23], 0\n\t" \
  "chr_w2:\n\t" \
  "s_memrealtime s[4:5]\n\t" \
  "s_waitcnt lgkmcnt(0)\n\t" \
  "s_sub_u32 s18, s4, s26\n\t" \
  "s_cmp_lt_i32 s18, 0\n\t" \
  "s_cbranch_scc1 chr_w2\n\t" \
  "buffer_load_dwordx4 v[32:35], v3, s[36:39], s25 offen sc1\n\t" \
  "s_sleep 3\n\t" \
  "v_mov_b64 v[36:37], -1\n\t" \
  "v_mov_b64 v[38:39], -1\n\t" \
  "buffer_store_dwordx4 v[36:39], v4, s[36:39], s66 offen sc1\n\t" \
  "s_add_u32 s69, s64, 2\n\t" \
  "s_and_b32 s69, s69, 3\n\t" \
  "s_lshl_b32 s69, s69, 15\n\t" \
  "s_add_u32 s69, s69, s52\n\t" \
  "buffer_store_dword v36, v9, s[36:39], s69 offen sc1\n\t" \
  "s_add_u32 s69, s69, 16384\n\t" \
  "s_mov_b64 exec, 1\n\t" \
  "buffer_store_dwordx2 v[36:37], v91, s[36:39], s69 offen sc1\n\t" \
  "s_mov_b64 exec, -1\n\t" \
  "s_add_u32 s18, s64, 1\n\t" \
  "v_mov_b32 v44, 0\n\t" \
  "v_mov_b32 v46, s18\n\t" \
  "v_mov_b32 v47, s96\n\t" \
  "s_mov_b64 exec, 1\n\t" \
  "global_store_dwordx2 v44, v[46:47], s[34:35] sc1\n\t" \
  "s_mov_b64 exec, -1\n\t" \
  "v_add_u32 v61, s11, v1\n\t" \
  "s_waitcnt vmcnt(4)\n\t" \
  "v_max_u32 v60, v32, v33\n\t" \
  "v_max3_u32 v60, v60, v34, v35\n\t" \
  "v_cmp_eq_u32 vcc, -1, v60\n\t" \
  "s_cbranch_vccnz chr_p2s\n\t" \
  "chr_p2e:\n\t" \
  "ds_write_b128 v61, v[32:35] offset:0\n\t" \
  "s_mov_b32 s18, s66\n\t" \
  "s_mov_b32 s66, s65\n\t" \
  "s_mov_b32 s65, s73\n\t" \
  "s_mov_b32 s73, s18\n\t" \
  "s_add_u32 s64, s64, 1\n\t" \
  "s_cmp_lt_u32 s64, 63\n\t" \
  "s_cbranch_scc1 chr_loop\n\t" \
  "s_waitcnt lgkmcnt(0)\n\t" \
  "s_barrier\n\t" \
  "ds_read_b128 v[32:35], v1 offset:0\n\t" \
  "ds_read_b128 v[36:39], v1 offset:1024\n\t" \
  "ds_read_b128 v[40:43], v1 offset:2048\n\t" \
  "ds_read_b128 v[44:47], v1 offset:3072\n\t" \
  "ds_read_b128 v[48:51], v1 offset:4096\n\t" \
  "ds_read_b128 v[52:55], v1 offset:5120\n\t" \
  "ds_read_b128 v[56:59], v1 offset:6144\n\t" \
  "ds_read_b128 v[60:63], v1 offset:7168\n\t" \
  "s_waitcnt lgkmcnt(7)\n\t" \
  "v_smfmac_f32_16x16x64_bf16 v[12:15], v[32:35], a[0:7], v0\n\t" \
  "v_smfmac_f32_16x16x64_bf16 v[16:19], v[32:35], a[8:15], v0\n\t" \
  "s_waitcnt lgkmcnt(6)\n\t" \
  "v_smfmac_f32_16x16x64_bf16 v[12:15], v[36:39], a[16:23], v0\n\t" \
  "v_smfmac_f32_16x16x64_bf16 v[16:19], v[36:39], a[24:31], v0\n\t" \
  "s_waitcnt lgkmcnt(5)\n\t" \
  "v_smfmac_f32_16x16x64_bf16 v[12:15], v[40:43], a[32:39], v0\n\t" \
  "v_smfmac_f32_16x16x64_bf16 v[16:19], v[40:43], a[40:47], v0\n\t" \
  "s_waitcnt lgkmcnt(4)\n\t" \
  "v_smfmac_f32_16x16x64_bf16 v[12:15], v[44:47], a[48:55], v0\n\t" \
  "v_smfmac_f32_16x16x64_bf16 v[16:19], v[44:47], a[56:63], v0\n\t" \
  "s_waitcnt lgkmcnt(3)\n\t" \
  "v_smfmac_f32_16x16x64_bf16 v[12:15], v[48:51], a[64:71], v0\n\t" \
  "v_smfmac_f32_16x16x64_bf16 v[16:19], v[48:51], a[72:79], v0\n\t" \
  "s_waitcnt lgkmcnt(2)\n\t" \
  "v_smfmac_f32_16x16x64_bf16 v[12:15], v[52:55], a[80:87], v0\n\t" \
  "v_smfmac_f32_16x16x64_bf16 v[16:19], v[52:55], a[88:95], v0\n\t" \
  "s_waitcnt lgkmcnt(1)\n\t" \
  "v_smfmac_f32_16x16x64_bf16 v[12:15], v[56:59], a[96:103], v0\n\t" \
  "v_smfmac_f32_16x16x64_bf16 v[16:19], v[56:59], a[104:111], v0\n\t" \
  "s_waitcnt lgkmcnt(0)\n\t" \
  "v_smfmac_f32_16x16x64_bf16 v[12:15], v[60:63], a[112:119], v0\n\t" \
  "v_smfmac_f32_16x16x64_bf16 v[16:19], v[60:63], a[120:127], v0\n\t" \
  "s_nop 6\n\t" \
  "v_add_f32 v12, v12, v14\n\t" \
  "v_add_f32 v13, v13, v15\n\t" \
  "v_add_f32 v16, v16, v18\n\t" \
  "v_add_f32 v17, v17, v19\n\t" \
  "v_cvt_pk_bf16_f32 v12, v12, v13\n\t" \
  "v_cvt_pk_bf16_f32 v16, v16, v17\n\t" \
  "ds_write2st64_b32 v2, v12, v16 offset1:1\n\t" \
  "ds_read_b128 v[12:15], v5 offset:24576\n\t" \
  "ds_read_b128 v[16:19], v5 offset:25600\n\t" \
  "v_smfmac_f32_16x16x64_bf16 v[20:23], v[32:35], v[64:71], v0\n\t" \
  "ds_read_b128 v[64:67], v5 offset:32768\n\t" \
  "ds_read_b128 v[68:71], v5 offset:33792\n\t" \
  "v_smfmac_f32_16x16x64_bf16 v[20:23], v[36:39], v[72:79], v0\n\t" \
  "ds_read_b128 v[72:75], v5 offset:40960\n\t" \
  "ds_read_b128 v[76:79], v5 offset:41984\n\t" \
  "ds_read_b128 v[32:35], v5 offset:49152\n\t" \
  "ds_read_b128 v[36:39], v5 offset:50176\n\t" \
  "v_smfmac_f32_16x16x64_bf16 v[20:23], v[40:43], v[80:87], v0\n\t" \
  "ds_read_b128 v[80:83], v5 offset:57344\n\t" \
  "ds_read_b128 v[84:87], v5 offset:58368\n\t" \
  "s_waitcnt lgkmcnt(8)\n\t" \
  "v_smfmac_f32_16x16x64_bf16 v[20:23], v[44:47], v[12:19], v0\n\t" \
  "s_waitcnt lgkmcnt(6)\n\t" \
  "v_smfmac_f32_16x16x64_bf16 v[20:23], v[48:51], v[64:71], v0\n\t" \
  "s_waitcnt lgkmcnt(4)\n\t" \
  "v_smfmac_f32_16x16x64_bf16 v[20:23], v[52:55], v[72:79], v0\n\t" \
  "s_waitcnt lgkmcnt(2)\n\t" \
  "v_smfmac_f32_16x16x64_bf16 v[20:23], v[56:59], v[32:39], v0\n\t" \
  "s_waitcnt lgkmcnt(0)\n\t" \
  "v_smfmac_f32_16x16x64_bf16 v[20:23], v[60:63], v[80:87], v0\n\t" \
  "s_nop 7\n\t" \
  "v_add_f32 v20, v20, v22\n\t" \
  "v_add_f32 v21, v21, v23\n\t" \
  "v_cvt_pk_bf16_f32 v20, v20, v21\n\t" \
  "ds_write_b32 v6, v20\n\t" \
  "s_waitcnt lgkmcnt(0)\n\t" \
  "s_lshl_b32 s18, s99, 8\n\t" \
  "v_add_u32 v40, s18, v2\n\t" \
  "ds_read_b32 v40, v40\n\t" \
  "ds_read_b32 v41, v6\n\t" \
  "v_subrev_u32 v42, s12, v2\n\t" \
  "v_subrev_u32 v43, s13, v6\n\t" \
  "s_waitcnt lgkmcnt(0)\n\t" \
  "s_lshl_b32 s69, s64, 16\n\t" \
  "s_add_u32 s9, s69, s60\n\t" \
  "s_add_u32 s69, s69, s61\n\t" \
  "buffer_store_dword v40, v42, s[44:47], s9 offen sc1\n\t" \
  "buffer_store_dword v41, v43, s[48:51], s69 offen sc1\n\t" \
  "s_waitcnt vmcnt(0)\n\t" \
  "s_barrier\n\t" \
  "v_mov_b32 v44, 0\n\t" \
  "v_mov_b32 v46, 64\n\t" \
  "v_mov_b32 v47, s96\n\t" \
  "s_mov_b64 exec, 1\n\t" \
  "global_store_dwordx2 v44, v[46:47], s[34:35] sc1\n\t" \
  "s_mov_b64 exec, -1\n\t" \
  "s_endpgm\n\t" \
  "chr_p2s:\n\t" \
  "s_cmp_lg_u32 s71, 0\n\t" \
  "s_cbranch_scc1 chr_p2e\n\t" \
  "chr_p2r:\n\t" \
  "s_mov_b64 exec, vcc\n\t" \
  "buffer_load_dwordx4 v[32:35], v3, s[36:39], s25 offen sc1\n\t" \
  "s_add_u32 s70, s70, 1\n\t" \
  "s_waitcnt vmcnt(0)\n\t" \
  "v_max_u32 v60, v32, v33\n\t" \
  "v_max3_u32 v60, v60, v34, v35\n\t" \
  "v_cmp_eq_u32 vcc, -1, v60\n\t" \
  "s_cbranch_vccz chr_p2d\n\t" \
  "s_cmp_lt_u32 s70, 200000\n\t" \
  "s_cbranch_scc1 chr_p2r\n\t" \
  "s_mov_b32 s71, 1\n\t" \
  "v_mov_b32 v60, 0\n\t" \
  "v_mov_b32 v32, 1\n\t" \
  "v_mov_b32 v33, s96\n\t" \
  "global_store_dwordx2 v60, v[32:33], s[62:63] sc1\n\t" \
  "chr_p2d:\n\t" \
  "s_mov_b64 exec, -1\n\t" \
  "s_branch chr_p2e\n\t" \
  "chr_ts:\n\t" \
  "s_cmp_lg_u32 s71, 0\n\t" \
  "s_cbranch_scc1 chr_te\n\t" \
  "buffer_load_dwordx2 v[88:89], v90, s[36:39], s14 offen sc1\n\t" \
  "s_add_u32 s70, s70, 1\n\t" \
  "s_waitcnt vmcnt(0)\n\t" \
  "s_cmp_lt_u32 s70, 200000\n\t" \
  "s_cbranch_scc1 chr_tc\n\t" \
  "s_mov_b32 s71, 1\n\t" \
  "s_branch chr_te\n\t" \
  "chr_p1s:\n\t" \
  "v_max3_u32 v28, v40, v41, v42\n\t" \
  "v_max_u32 v28, v28, v43\n\t" \
  "v_max3_u32 v28, v28, v44, v45\n\t" \
  "v_max3_u32 v28, v28, v46, v47\n\t" \
  "v_max3_u32 v28, v28, v48, v49\n\t" \
  "v_max3_u32 v28, v28, v50, v51\n\t" \
  "v_max3_u32 v28, v28, v52, v53\n\t" \
  "v_max3_u32 v28, v28, v54, v55\n\t" \
  "v_max3_u32 v28, v28, v56, v57\n\t" \
  "v_max3_u32 v28, v28, v58, v59\n\t" \
  "v_max3_u32 v28, v28, v60, v61\n\t" \
  "v_max3_u32 v28, v28, v62, v63\n\t" \
  "v_max3_u32 v28, v28, v80, v81\n\t" \
  "v_max3_u32 v28, v28, v82, v83\n\t" \
  "v_max3_u32 v28, v28, v84, v85\n\t" \
  "v_max3_u32 v28, v28, v86, v87\n\t" \
  "v_cmp_eq_u32 vcc, -1, v28\n\t" \
  "s_cbranch_vccz chr_p1e\n\t" \
  "s_cmp_lg_u32 s71, 0\n\t" \
  "s_cbranch_scc1 chr_p1e\n\t" \
  "chr_p1r:\n\t" \
  "s_mov_b64 exec, vcc\n\t" \
  "buffer_load_dwordx4 v[40:43], v8, s[36:39], s69 offen offset:0 sc1\n\t" \
  "buffer_load_dwordx4 v[44:47], v8, s[36:39], s69 offen offset:1024 sc1\n\t" \
  "buffer_load_dwordx4 v[48:51], v8, s[36:39], s69 offen offset:2048 sc1\n\t" \
  "buffer_load_dwordx4 v[52:55], v8, s[36:39], s69 offen offset:3072 sc1\n\t" \
  "buffer_load_dwordx4 v[56:59], v8, s[36:39], s9 offen offset:0 sc1\n\t" \
  "buffer_load_dwordx4 v[60:63], v8, s[36:39], s9 offen offset:1024 sc1\n\t" \
  "buffer_load_dwordx4 v[80:83], v8, s[36:39], s9 offen offset:2048 sc1\n\t" \
  "buffer_load_dwordx4 v[84:87], v8, s[36:39], s9 offen offset:3072 sc1\n\t" \
  "s_add_u32 s70, s70, 1\n\t" \
  "s_waitcnt vmcnt(0)\n\t" \
  "v_max3_u32 v28, v40, v44, v48\n\t" \
  "v_max3_u32 v29, v52, v56, v60\n\t" \
  "v_max3_u32 v28, v28, v80, v84\n\t" \
  "v_max_u32 v28, v28, v29\n\t" \
  "v_cmp_eq_u32 vcc, -1, v28\n\t" \
  "s_cbranch_vccz chr_p1d\n\t" \
  "s_cmp_lt_u32 s70, 200000\n\t" \
  "s_cbranch_scc1 chr_p1r\n\t" \
  "s_mov_b32 s71, 1\n\t" \
  "v_mov_b32 v28, 0\n\t" \
  "v_mov_b32 v30, 1\n\t" \
  "v_mov_b32 v31, s96\n\t" \
  "global_store_dwordx2 v28, v[30:31], s[62:63] sc1\n\t" \
  "chr_p1d:\n\t" \
  "s_mov_b64 exec, -1\n\t" \
  "s_nop 3\n\t" \
  "s_branch chr_p1m\n\t" \
  ""

DEV bool give_up(const Args& A, int sp) {
  if ((sp & 63) != 63 || (sp <= SPIN_MAX && !sgpr(ld_flag(A.ctrl + C_ABORT, A.tag)))) return false;
  st_flag(A.ctrl + C_ABORT, 1u, A.tag); return true;
}
DEV unsigned wait_ge(const Args& A, unsigned* p, unsigned v, int nap) {
  for (int sp = 0;; ++sp) {
    unsigned x = ~0u;
    if ((threadIdx.x & 63) < 4) x = ld_flag(p, A.tag);
    x = min(min(__builtin_amdgcn_readlane(x, 0), __builtin_amdgcn_readlane(x, 1)), min(__builtin_amdgcn_readlane(x, 2), __builtin_amdgcn_readlane(x, 3)));
    if (x >= v) return x;
    if (x + 1 + (nap >> 1) < v) __builtin_amdgcn_s_sleep(32); else if (nap & 1) __builtin_amdgcn_s_sleep(6); else __builtin_amdgcn_s_sleep(1);
    if (give_up(A, sp)) return ~0u;
  }
}
DEV unsigned near_pages(const Args& A) {
  const int l = threadIdx.x & 63;
  for (int sp = 0;; ++sp) {
    const unsigned c = ld_flag(A.ctrl + C_CAL + 2 * (l < 6 ? l : 0), A.tag);
    if (!__any(c == 0) || give_up(A, sp))
      return (__builtin_amdgcn_readlane(c, 0) <= __builtin_amdgcn_readlane(c, 1) ? 0u : 1u) | (__builtin_amdgcn_readlane(c, 2) <= __builtin_amdgcn_readlane(c, 3) ? 0u : 2u)
           | (__builtin_amdgcn_readlane(c, 4) <= __builtin_amdgcn_readlane(c, 5) ? 0u : 4u);
    __builtin_amdgcn_s_sleep(4);
  }
}
DEV void init_lines(const Args& A, const int i) {
  const unsigned t = threadIdx.x, par = near_pages(A);
  const __amdgpu_buffer_rsrc_t rh = mk_rsrc(A.ws + WS_HOP, 3 * REGION);
  const unsigned o1 = page_off(H1_PAGE + 2 * i) + t * 16, o2 = i < 8 ? page_off(2 * i) + t * 16 : 0x80000000u;
  u32x4 z[4];
#pragma unroll
  for (unsigned r = 0; r < 3; ++r) z[r] = bld16(rh, o1, r * REGION + ((par >> r) & 1) * 8192);
  z[3] = bld16(rh, o2, (par & 1) * 8192);
  asm volatile("s_waitcnt vmcnt(0)" : "+v"(z[0]), "+v"(z[1]), "+v"(z[2]), "+v"(z[3]) :: "memory");
  const u32x4 sent4 = {SENT, SENT, SENT, SENT};
#pragma unroll
  for (unsigned r = 0; r < 3; ++r) bst16(sent4, rh, o1, r * REGION + ((par >> r) & 1) * 8192);
  bst16(sent4, rh, o2, (par & 1) * 8192);
  vm_wait0(); raw_barrier();
  if (t == 0) st_flag(A.ctrl + C_INIT + 2 * i, 1u, A.tag);
}
#define NCAL 6
DEV void calibrate(const Args& A, const int c) {
  if (threadIdx.x >= 64) return;
  const int l = threadIdx.x;
  const __amdgpu_buffer_rsrc_t rh = mk_rsrc(A.ws + WS_HOP, 3 * REGION);
  const unsigned off = (c >> 1) * REGION + (1u << 20) + (1u << 19) + (c & 1) * 8192;
  unsigned z = __builtin_amdgcn_raw_buffer_load_b32(rh, l < NCAL ? off + l * 256 : 0x80000000u, 0, AUX_SC1);
  asm volatile("s_waitcnt vmcnt(0)" : "+v"(z) :: "memory");
  if (l < NCAL) __builtin_amdgcn_raw_buffer_store_b32(0u, rh, off + l * 256, 0, AUX_SC1);
  vm_wait0();
  unsigned best = 0xFFFFFFFu;
  for (int r = 0; r < NCAL; ++r) {
    const unsigned long long t0 = __builtin_amdgcn_s_memtime();
    __builtin_amdgcn_s_waitcnt(0xC07F);
    unsigned v = __builtin_amdgcn_raw_buffer_load_b32(rh, off + r * 256, 0, AUX_SC1);
    asm volatile("s_waitcnt vmcnt(0)" : "+v"(v) :: "memory");
    const unsigned long long t1 = __builtin_amdgcn_s_memtime();
    __builtin_amdgcn_s_waitcnt(0xC07F);
    best = min(best, (unsigned)(t1 - t0));
  }
  if (l == 0) st_flag(A.ctrl + C_CAL + 2 * c, best + 1, A.tag);
}
DEV void chain_role(const Args& A, const int role, ldsp L) {
  const int t = threadIdx.x, l = t & 63, g = l >> 4, c16 = l & 15;
  const int w = __builtin_amdgcn_readfirstlane(t >> 6);
  const int wl = w & 3, whi = w >> 2;
  const int h = role >> 1, j = role & 1;
  unsigned* ctrl = A.ctrl;
  const __amdgpu_buffer_rsrc_t rh = mk_rsrc(A.ws + WS_HOP, 3 * REGION);
  constexpr unsigned INBOX = 4 * 4096;
  unsigned h1st = page_off(H1_PAGE) + (unsigned)(w * 8 + (l >> 3)) * INBOX + role * 128 + (l & 7) * 16;
  const unsigned s_h1ld = page_off(H1_PAGE) + role * INBOX;
  unsigned h2st = c16 < 8 ? role * 128 + (g & 1) * 64 + (g >> 1) * 32 + c16 * 4 : 0x80000000u;
  constexpr int RW = 7;
  const unsigned l16 = l * 16;
  bf16x16 wqk[2][8], wo[4];
  const unsigned q_ = (l >> 2) & 3, p_ = l & 3;
  {
    bf16x8 wo_lo[4];
    unsigned oA[2], oO[2], oV[2];
#pragma unroll
    for (int n = 0; n < 2; ++n) {
      { const unsigned row = w * 8 + n * 4 + (l >> 4); oA[n] = row * (HD * 2) + ((l & 15) ^ (swz_f(row) << 1)) * 16; }
      { const unsigned row = w * 2 + n; oO[n] = row * (DM * 2) + (l ^ ((row & 1) | (((row >> 3) & 1) << 3))) * 16; }
      { const unsigned row = w * 16 + n * 8 + (l >> 3); oV[n] = row * (HD * 2) + ((l & 7) ^ (swz_v(row) << 1)) * 16; }
    }
    auto issue = [&](int b) {
      const unsigned dst = (unsigned)(size_t)L + L_ST + (b & 7) * 16384 + w * 2048;
      const char* base = b < 16 ? (const char*)(((b & 1) ? A.Wk : A.Wq) + (size_t)((b >> 1) ^ (4 * j)) * 64 * HD + h * DH)
                       : b < 20 ? (const char*)(A.Wo + ((size_t)h * DH + j * 64 + (b - 16) * 16) * DM)
                                : (const char*)(A.Wv + (size_t)((b - 20) ^ (2 * j)) * 128 * HD + h * DH + j * 64);
#pragma unroll
      for (int n = 0; n < 2; ++n) {
        glds(base, b < 16 ? oA[n] : b < 20 ? oO[n] : oV[n], dst + n * 1024);
      }
    };
    const unsigned rowT = 16 * (g >> 1) + 4 * (g & 1) + (q_ & 1) + 8 * (q_ >> 1);
    const unsigned rowN = 8 * g + q_;
    auto rdW = [&](int slot, int i) -> bf16x16 {
      ldsp b = L + L_ST + slot * 16384 + rowN * 256 + ((2 * ((2 * wl + i) ^ swz_f(rowN)) + (p_ >> 1)) * 16) + (p_ & 1) * 8;
      return cat16(cat8(tr_read(b), tr_read(b + 1024)), cat8(tr_read(b + 8192), tr_read(b + 8192 + 1024)));
    };
    auto rdO = [&](int slot, int T) -> bf16x8 {
      const unsigned cb = (64 * w + 16 * p_ + 4 * T) * 2;
      const unsigned sw = (rowT & 1) | (((rowT >> 3) & 1) << 3);
      ldsp b = L + L_ST + slot * 16384 + rowT * 1024 + (((cb >> 4) ^ sw) << 4) + (cb & 15);
      return cat8(tr_read(b), tr_read(b + 2048));
    };
    auto rdV = [&](int slot) -> bf16x16 {
      ldsp b = L + L_ST + slot * 16384 + whi * 8192 + rowN * 128 + ((2 * (wl ^ swz_v(rowN)) + (p_ >> 1)) * 16) + (p_ & 1) * 8;
      return cat16(cat8(tr_read(b), tr_read(b + 512)), cat8(tr_read(b + 4096), tr_read(b + 4096 + 512)));
    };
#pragma unroll
    for (int b = 0; b < 8; ++b) issue(b);
#pragma unroll
    for (int b = 1; b < 24; b += 2) {
      if (b == 1) vm_wait<12>(); else if (b <= 19) vm_wait<8>();
      else if (b == 21) vm_wait<4>(); else vm_wait<0>();
      raw_barrier();
      const int slot = b & 7;
      if (b < 16) {
        wqk[0][b >> 1] = rdW(slot - 1 + whi, 0); wqk[1][b >> 1] = rdW(slot - 1 + whi, 1);
        if (b >= 3) { issue(b + 5); issue(b + 6); }
        asm volatile("" : "+a"(wqk[0][b >> 1]), "+a"(wqk[1][b >> 1]));
      } else if (b == 17) {
#pragma unroll
        for (int T = 0; T < 4; ++T) wo_lo[T] = rdO(0, T);
        issue(22); issue(23);
#pragma unroll
        for (int T = 0; T < 4; ++T) PIN1(wo_lo[T]);
      } else if (b == 19) {
#pragma unroll
        for (int T = 0; T < 4; ++T) { wo[T] = cat16(wo_lo[T], rdO(2, T)); PIN1(wo[T]); }
      } else {
        const bf16x16 x0 = rdV(slot - 1), x1 = rdV(slot);
        raw_barrier();
        ldsp d = L + L_ST + (slot - 1) * 16384 + w * 2048 + l16;
        *(LAS bf16x8*)(d) = __builtin_shufflevector(x0, x0, 0, 1, 2, 3, 4, 5, 6, 7); *(LAS bf16x8*)(d + 1024) = __builtin_shufflevector(x0, x0, 8, 9, 10, 11, 12, 13, 14, 15);
        *(LAS bf16x8*)(d + 16384) = __builtin_shufflevector(x1, x1, 0, 1, 2, 3, 4, 5, 6, 7); *(LAS bf16x8*)(d + 16384 + 1024) = __builtin_shufflevector(x1, x1, 8, 9, 10, 11, 12, 13, 14, 15);
      }
    }
  }
  const unsigned lb = (unsigned)(size_t)L;
  const unsigned a_l16 = lb + l16;
  const __amdgpu_buffer_rsrc_t rkv = mk_rsrc(A.ws + (wl == 2 ? WS_VX : WS_KX) + (size_t)h * S_TOK * 256, S_TOK * 256);
  const unsigned kv_vo = wl == 2 ? (l16 >> 7) * 256 + (l16 & 127) : l16;
  const unsigned kv_so = wl == 2 ? j * 128 : wl * 4096, kv_st = wl == 2 ? 2048 : 1024;
  const unsigned kv_b0 = wl < 2 ? lb + L_KX + ((l >> 2) & 3) * 2048 + (16 * wl + g) * 64 + ((l & 3) << 4) : lb + L_VX + ((l & 7) >> 1) * 1024 + (l >> 3) * 32 + (l & 1) * 16;
  const unsigned kv_b1 = wl < 2 ? kv_b0 ^ 32 : kv_b0;
  unsigned dead = 0, par;
  unsigned* kvprog = ctrl + C_PP + h * 32;
  if (whi && w != RW) {
    if (wait_ge(A, kvprog, 1u, 0) == ~0u) dead = 1;
#pragma unroll
    for (int n = 0; n < 4; ++n) *(LAS u32x4*)LP(((n & 2) ? kv_b1 : kv_b0) + n * 256) = bld16(rkv, kv_vo, kv_so + n * kv_st);
  }
  { const unsigned* m0 = (const unsigned*)(A.mem0 + (2 * ((l >> 2) & 3) + (l & 1)) * DM + 64 * w + 16 * g + (l & 2));
    *(LAS u32x4*)(L + L_MEM + (w ^ (4 * j)) * 1024 + l16) = u32x4{m0[0], m0[2], m0[4], m0[6]}; }
  par = near_pages(A);
  for (int sp = 0;; ++sp) {
    if (!__any(ld_flag(ctrl + C_INIT + 2 * l, A.tag) == 0)) break;
    __builtin_amdgcn_s_sleep(2);
    if (give_up(A, sp)) { dead = 1; break; }
  }
  uint64_t dump = 0;
  const u32x4 s_rh = rsrc_words(A.ws + WS_HOP, 3 * REGION), s_rkv = rsrc_words(A.ws + (wl == 2 ? WS_VX : WS_KX) + (size_t)h * S_TOK * 256, S_TOK * 256);
  const u32x4 s_rkm = rsrc_words(A.ws + WS_KM, 4u << 20), s_rvm = rsrc_words(A.ws + WS_VM, 4u << 20);
  const uint64_t abortp = (uint64_t)(ctrl + C_ABORT);
  const u32x8 s_p0 = {(par & 1) * 8192, REGION + ((par >> 1) & 1) * 8192, 2 * REGION + ((par >> 2) & 1) * 8192, (unsigned)w * 1024, ((unsigned)A.d1 & 0xFFFFu) | ((unsigned)A.d2 << 16), wl < 2 ? 32u : 0u, kv_so, kv_st};
  const u32x4 s_p1 = {(unsigned)h * 2048 + j * 256, (unsigned)h * 2048 + j * 1024, (unsigned)abortp, (unsigned)(abortp >> 32)};
  const uint64_t kvp = (uint64_t)kvprog, mp = (uint64_t)(ctrl + C_PM + role * 32);
  const u32x4 s_p2 = {(unsigned)role * 128, lb, (unsigned)kvp, (unsigned)(kvp >> 32)};
  const unsigned a_qkw = lb + L_QT + whi * 2048 + (32 * wl + c16) * 16 + g * 4;
  const unsigned vtw = lb + L_VT + (16 * wl + c16) * 16 + g * 4;
  u32x4 va, vb, vc, vsel;
  va = u32x4{(c16 & 2) ? 0xEEEEu : 0x4444u, a_l16, a_qkw, (((l >> 2) & 3) * 2 + (g >> 1)) * 128u + ((g & 1) * 4 + (l & 3)) * 16u};
  if (!whi) {
    vb = u32x4{h1st, lb + (8 * g + q_) * 16 + (p_ & 1) * 8, lb + L_KX + c16 * 64 + ((g ^ (((c16 >> 3) & 1) << 1)) << 4), lb + L_VX + wl * 1024 + (4 * g + q_) * 32 + p_ * 8};
    vc = u32x4{lb + L_OI + wl * 256 + (4 * ((c16 & 7) >> 1) + 2 * (g >> 1) + (c16 & 1)) * 16 + (g & 1) * 8, lb + L_VT + (16 * wl + c16) * 16 + (g & 1) * 8, ubits(g < 2 ? 0.f : 1.0e30f), 0u};
  } else {
    vb = u32x4{h1st, lb + L_WV + wl * 2048 + l16, vtw, 0u};
    vc = u32x4{w == RW ? l16 : kv_vo, w == RW ? h2st : kv_b0, 0u, 0u};
  }
  { const unsigned e = c16 & 7, v = (e & 1) ? 0x3F800000u : 0x00003F80u, ee = e >> 1; vsel = u32x4{ee == 0 ? v : 0u, ee == 1 ? v : 0u, ee == 2 ? v : 0u, ee == 3 ? v : 0u}; }
  const u32x32 w0 = catw(catw(wqk[0][0], wqk[1][0]), catw(wqk[0][1], wqk[1][1])), w1 = catw(catw(wqk[0][2], wqk[1][2]), catw(wqk[0][3], wqk[1][3]));
  const u32x32 w2 = catw(catw(wqk[0][4], wqk[1][4]), catw(wqk[0][5], wqk[1][5])), w3 = catw(catw(wqk[0][6], wqk[1][6]), catw(wqk[0][7], wqk[1][7]));
  const u32x32 w4 = catw(catw(wo[0], wo[1]), catw(wo[2], wo[3]));
#define CHAIN_OPERANDS : : "{a[0:31]}"(w0), "{a[32:63]}"(w1), "{a[64:95]}"(w2), "{a[96:127]}"(w3), "{v[96:127]}"(w4), "{v[0:3]}"(va), "{v[4:7]}"(vb), "{v[8:11]}"(vc), "{v[24:27]}"(vsel), \
    "{s[36:39]}"(s_rh), "{s[40:43]}"(s_rkv), "{s[44:47]}"(s_rkm), "{s[48:51]}"(s_rvm), "{s[52:59]}"(s_p0), "{s[60:63]}"(s_p1), "{s[28:31]}"(s_p2), "{s[34:35]}"(mp), \
    "{s71}"(sgpr(dead)), "{s[96:99]}"(u32x4{A.tag, 0u, s_h1ld, (unsigned)j}), "{s[100:101]}"(dump) : "memory"
  if (!whi) asm volatile(CHAIN_A_ASM CHAIN_OPERANDS);
  else if (w != RW) asm volatile(CHAIN_B_ASM CHAIN_OPERANDS);
  else asm volatile(CHAIN_R_ASM CHAIN_OPERANDS);
  __builtin_unreachable();
}

DEV int get_slot(unsigned* ctrl, ldsp L, unsigned& tag) {
  LAS unsigned* sh = (LAS unsigned*)(L + L_MISC + 64);
  if (threadIdx.x == 0) {
    const unsigned tg = ld_agent(ctrl + C_EPOCH) + 1u;
    unsigned slot = (xcc_id() & 7) * 32 + ((blockIdx.x >> 3) & 31);
    for (int n = 0; n < 256; ++n, slot = (slot + 1) & 255)
      if (__hip_atomic_exchange(ctrl + C_SLOT + slot * 16, tg, __ATOMIC_RELAXED, __HIP_MEMORY_SCOPE_AGENT) != tg) break;
    sh[0] = slot; sh[1] = tg;
  }
  __syncthreads();
  tag = sgpr(sh[1]);
  return sgpr(sh[0]);
}
DEV void close_epoch(const Args& A) {
  const int l = threadIdx.x & 63;
  for (int sp = 0;; ++sp) {
    bool ok = true;
#pragma unroll
    for (int n = 0; n < 4; ++n) ok &= ld_agent(A.ctrl + C_SLOT + (l + 64 * n) * 16) == A.tag;
    if (__all(ok) || give_up(A, sp)) break;
    __builtin_amdgcn_s_sleep(8);
  }
  if (l == 0) st_agent(A.ctrl + C_EPOCH, A.tag);
}

constexpr int P_X = 0, P_OUT = 131072;
#define MF32_0(acc, a, b) asm volatile("v_mfma_f32_32x32x16_bf16 %0, %1, %2, 0" : "=&v"(acc) : "a"(a), "v"(b))
#define MF32(acc, a, b) asm volatile("v_mfma_f32_32x32x16_bf16 %0, %1, %2, %0" : "+v"(acc) : "a"(a), "v"(b))
#define NE 6
template <int KIND>
DEV void proj_role(const Args& A, const int p, ldsp L) {
  const int t = threadIdx.x, l = t & 63;
  const int w = __builtin_amdgcn_readfirstlane(t >> 6), cw = w & 3, sh = w >> 2;
  const unsigned lb = (unsigned)(size_t)L;
  const bf16 *W0, *W1; int ldw;
  if (KIND == 0) { W0 = p < 32 ? A.Wk + p * DH : A.Wq + (p - 32) * 2 * DH; W1 = p < 32 ? A.Wv + p * DH : W0 + DH; ldw = HD; }
  else { W0 = A.w1 + (p - 48) * 256; W1 = W0 + 128; ldw = FF; }
  unsigned* prog = A.ctrl + C_PP + p * 32;
  const unsigned xoff = l * 16;
  auto issue1 = [&](int it, int n) {
    const int row = w * 4 + n;
    glds(A.x + (size_t)(it * 32 + row) * DM, xoff ^ ((row & 15) << 4), lb + P_X + (it & 3) * 32768 + row * 1024);
  };
  auto issue = [&](int it) {
#pragma unroll
    for (int n = 0; n < 4; ++n) issue1(it, n);
  };
  const unsigned tok = l & 31, hi = l >> 5;
  unsigned xb[8];
#pragma unroll
  for (int k = 0; k < 8; ++k) { xb[k] = lb + P_X + tok * 1024 + (((2 * k + hi) ^ (tok & 15)) << 4); PIN1(xb[k]); }
  f32x16 acc;
  bf16x8 wf[32];
  {
    unsigned off[2];
#pragma unroll
    for (int n = 0; n < 2; ++n) { const unsigned row = w * 8 + n * 4 + (l >> 4); off[n] = row * ldw * 2 + (((l & 15) ^ ((row & 3) << 2)) << 4); }
    auto wissue = [&](int b) {
#pragma unroll
      for (int n = 0; n < 2; ++n) glds(((b & 1) ? W1 : W0) + (size_t)(b >> 1) * 64 * ldw, off[n], lb + (b & 7) * 16384 + w * 2048 + n * 1024);
    };
    const unsigned q = (l >> 2) & 3, pp = l & 3;
    const unsigned base = lb + sh * 16384 + (8 * (l >> 5) + q) * 256 + (((4 * cw + 2 * ((l >> 4) & 1) + (pp >> 1)) ^ (q << 2)) << 4) + (pp & 1) * 8;
#pragma unroll
    for (int n = 0; n < 4; ++n) { const int row = w * 4 + n; glds(A.x + (size_t)row * DM, xoff ^ ((row & 15) << 4), lb + P_OUT + row * 1024); }
#pragma unroll
    for (int b = 0; b < 8; ++b) wissue(b);
#pragma unroll
    for (int b = 1; b < 16; b += 2) {
      if (b == 1) vm_wait<12>(); else if (b <= 11) vm_wait<8>(); else if (b == 13) vm_wait<0>(); else {}
      raw_barrier();
      bf16x8 x0[4];
#pragma unroll
      for (int k4 = 0; k4 < 4; ++k4) {
        const unsigned a = base + ((b - 1) & 7) * 16384 + k4 * 4096;
        wf[(b >> 1) * 4 + k4] = cat8(tr_read(LP(a)), tr_read(LP(a + 1024)));
        x0[k4] = *(LAS bf16x8*)LP(xb[((b >> 1) * 4 + k4) & 7] + P_OUT + (b >> 2) * 256);
      }
      if (b >= 3 && b + 5 < 16) { wissue(b + 5); wissue(b + 6); }
      if (b == 13) issue(1);
      if (b == 15) issue(2);
#pragma unroll
      for (int k4 = 0; k4 < 4; ++k4) { if (b == 1 && k4 == 0) MF32_0(acc, wf[0], x0[0]); else MF32(acc, wf[(b >> 1) * 4 + k4], x0[k4]); }
    }
    asm volatile("s_nop 11" : "+v"(acc));
    raw_barrier();
  }
  const size_t ob = p < 32 ? (sh ? WS_VX : WS_KX) + (size_t)p * S_TOK * 256 : WS_QX + (size_t)((p - 32) * 2 + sh) * S_TOK * 256;
  const __amdgpu_buffer_rsrc_t ro = KIND == 0 ? mk_rsrc(A.ws + ob, S_TOK * 256) : mk_rsrc(A.ws + WS_H, 8u << 20);
  auto compute = [&](int it) {
    const unsigned bo = (it & 3) * 32768;
    bf16x8 xf[32];
#define XF(ks) (*(LAS bf16x8*)LP(xb[(ks) & 7] + bo + ((ks) >> 3) * 256))
#pragma unroll
    for (int ks = 0; ks < 4; ++ks) xf[ks] = XF(ks);
#pragma unroll
    for (int ks = 0; ks < 32; ++ks) {
      if (ks == 0) MF32_0(acc, wf[0], xf[0]); else MF32(acc, wf[ks], xf[ks]);
      if (ks + 4 < 32) xf[ks + 4] = XF(ks + 4);
      if ((ks & 3) == 2 && ks < 16) issue1(min(it + 3, NSEG - 1), ks >> 2);
    }
    asm volatile("s_nop 11" : "+v"(acc));
  };
  auto epilogue = [&](int it) {
    if (KIND == 0) {
      const unsigned ob = lb + P_OUT + w * 2048;
#pragma unroll
      for (int qd = 0; qd < 4; ++qd)
        *(LAS u32x2*)LP(ob + tok * 64 + ((qd ^ ((tok >> 1) & 3)) << 4) + hi * 8) = u32x2{pack2(acc[4 * qd], acc[4 * qd + 1]), pack2(acc[4 * qd + 2], acc[4 * qd + 3])};
#pragma unroll
      for (int n = 0; n < 2; ++n) {
        const unsigned row = n * 16 + (l >> 2);
        bst16(*(LAS u32x4*)LP(ob + row * 64 + (((l & 3) ^ ((row >> 1) & 3)) << 4)), ro, row * 256 + cw * 64 + (l & 3) * 16, it * 8192);
      }
    } else {
      unsigned hv[8];
#pragma unroll
      for (int r = 0; r < 8; ++r) {
        const float a = (float)(bf16)acc[2 * r], b = (float)(bf16)acc[2 * r + 1];
        hv[r] = pack2(a * __builtin_amdgcn_rcpf(1.0f + __expf(-a)), b * __builtin_amdgcn_rcpf(1.0f + __expf(-b)));
      }
      const unsigned fo = ((it * 2 + (tok >> 4)) * 56 + (p - 48) * 8 + w) * 1024 + (hi * 16 + (tok & 15)) * 16;
      bst16(u32x4{hv[0], hv[1], hv[2], hv[3]}, ro, fo, 0); bst16(u32x4{hv[4], hv[5], hv[6], hv[7]}, ro, fo + 512, 0);
    }
  };
  for (int it = 0; it < NE; ++it) {
    if (it) compute(it);
    epilogue(it);
    vm_wait0(); raw_barrier();
    if (t == 0) st_flag(prog, it + 1, A.tag);
    if (it == 0) issue(3);
  }
  if (sh == 0) {
    for (int it = NE; it < NSEG; ++it) {
      vm_wait<6>(); raw_barrier();
      if (t == 0 && it > NE + 1) st_flag(prog, it - 1, A.tag);
      compute(it); epilogue(it);
    }
  } else {
    vm_wait<4>(); raw_barrier(); compute(NE);
    for (int it = NE + 1; it < NSEG; ++it) {
      vm_wait<4>(); raw_barrier();
      epilogue(it - 1); compute(it);
    }
    epilogue(NSEG - 1);
  }
  vm_wait0(); raw_barrier();
  if (t == 0) st_flag(prog, (unsigned)NSEG, A.tag);
}

DEV void warm(const bf16* base, unsigned stride0, size_t stride1, ldsp L) {
  const int l = threadIdx.x & 63, w = __builtin_amdgcn_readfirstlane(threadIdx.x >> 6);
#pragma unroll
  for (int n = 0; n < 16; ++n) glds((const char*)base + (w >> 2) * stride1 + (size_t)((w & 3) * 64 + n * 4) * stride0, (l >> 4) * stride0 + (l & 15) * 16, (unsigned)(size_t)L + w * 16384 + n * 1024);
  vm_wait0();
}
DEV void nap(int ticks) { const unsigned long long t0 = __builtin_amdgcn_s_memrealtime(); while ((long long)(__builtin_amdgcn_s_memrealtime() - t0) < ticks) __builtin_amdgcn_s_sleep(16); }

DEV void gate_role(const Args& A, const int i) {
  const int t = threadIdx.x, l = t & 63;
  const __amdgpu_buffer_rsrc_t rg = mk_rsrc(A.ws + WS_G, S_TOK * 4);
  bf16x8 gv[4];
#pragma unroll
  for (int n = 0; n < 4; ++n) gv[n] = *(const bf16x8*)(A.gw + ((l & 15) + 16 * n) * 8);
#pragma unroll 2
  for (int s = i; s < NSEG; s += 8) {
    const int tokn = s * 32 + (t >> 4);
    float gs = 0.f;
#pragma unroll
    for (int n = 0; n < 4; ++n) {
      const bf16x8 xv = *(const bf16x8*)(A.x + (size_t)tokn * DM + ((l & 15) + 16 * n) * 8);
#pragma unroll
      for (int e = 0; e < 8; ++e) gs += (float)(bf16)((float)xv[e] * (float)gv[n][e]);
    }
#pragma unroll
    for (int o = 8; o; o >>= 1) gs += __shfl_xor(gs, o);
    gs = (float)(bf16)gs; gs = (float)(bf16)(1.0f / (1.0f + __expf(-gs)));
    if ((l & 15) == 0) __builtin_amdgcn_raw_buffer_store_b32(ubits(gs), rg, tokn * 4, 0, AUX_SC1);
  }
  vm_wait0(); raw_barrier();
  if (t == 0) st_flag(A.ctrl + C_PG + i * 32, 1u, A.tag);
}

#define TSTAMP(i, idx) do {} while (0)
#define MFA0(d, a, b) asm volatile("v_mfma_f32_16x16x32_bf16 %0, %1, %2, 0" : "=&v"(d) : "a"(a), "v"(b))
#define MFA(d, a, b) asm volatile("v_mfma_f32_16x16x32_bf16 %0, %1, %2, %0" : "+v"(d) : "a"(a), "v"(b))
#define MFV0(d, a, b) asm volatile("v_mfma_f32_16x16x32_bf16 %0, %1, %2, 0" : "=&v"(d) : "v"(a), "v"(b))
#define MFV(d, a, b) asm volatile("v_mfma_f32_16x16x32_bf16 %0, %1, %2, %0" : "+v"(d) : "v"(a), "v"(b))
DEV void attn_role(const Args& A, const int h, ldsp L) {
  const int t = threadIdx.x, l = t & 63, g = l >> 4, c16 = l & 15, q_ = (l >> 2) & 3, p_ = l & 3;
  const int w = __builtin_amdgcn_readfirstlane(t >> 6), qt = w & 1, cls = w >> 1;
  const unsigned lb = (unsigned)(size_t)L + w * 16384;
  unsigned* ctrl = A.ctrl;
  const __amdgpu_buffer_rsrc_t rq = mk_rsrc(A.ws + WS_QX + (size_t)h * S_TOK * 256, S_TOK * 256), rk = mk_rsrc(A.ws + WS_KX + (size_t)h * S_TOK * 256, S_TOK * 256),
                               rv = mk_rsrc(A.ws + WS_VX + (size_t)h * S_TOK * 256, S_TOK * 256), rkm = mk_rsrc(A.ws + WS_KM, 4u << 20), rvm = mk_rsrc(A.ws + WS_VM, 4u << 20), ro = mk_rsrc(A.ws + WS_O, 16u << 20);
  const float cexp = 0.08838834764831845f * 1.4426950408889634f;
  const f32x4 z4 = {0, 0, 0, 0};
  u32x4 ones8 = {0x3F803F80u, 0x3F803F80u, 0x3F803F80u, 0x3F803F80u};
  PIN1(ones8);
  const int qi = qt * 16 + c16;
  unsigned* pP = ctrl + C_PP + ((l & 1) ? 32 + (h >> 1) : h) * 32;
  unsigned* pM = ctrl + C_PM + (2 * h + (l & 1)) * 32;
  unsigned* pA = ctrl + C_PA + ((cls * 8 + (h >> 2)) * 2 + qt) * 32 + (h & 3) * 2;
  unsigned seenP = 0, seenM = 0;
  for (int s = cls; s < NSEG; s += 4) {
    if (seenP < (unsigned)s + 1) seenP = wait_ge(A, pP, s + 1, 1);
    asm volatile("" ::: "memory");
    u32x4 qf[4], kx[2][4], vr[8];
#pragma unroll
    for (int ks = 0; ks < 4; ++ks) {
      qf[ks] = bld16(rq, qi * 256 + g * 16 + ks * 64, s * 8192);
      kx[0][ks] = bld16(rk, c16 * 256 + g * 16 + ks * 64, s * 8192); kx[1][ks] = bld16(rk, c16 * 256 + g * 16 + ks * 64, s * 8192 + 4096);
    }
#pragma unroll
    for (int n = 0; n < 8; ++n) vr[n] = bld16(rv, l * 16 + (n & 3) * 1024, s * 8192 + (n >> 2) * 4096);
    f32x4 sx0 = z4, sx1 = z4, sm;
#pragma unroll
    for (int ks = 0; ks < 4; ++ks) { sx0 = mfma16(__builtin_bit_cast(bf16x8, kx[0][ks]), __builtin_bit_cast(bf16x8, qf[ks]), sx0); sx1 = mfma16(__builtin_bit_cast(bf16x8, kx[1][ks]), __builtin_bit_cast(bf16x8, qf[ks]), sx1); }
#pragma unroll
    for (int n = 0; n < 8; ++n) *(LAS u32x4*)LP(lb + (c16 >> 1) * 1024 + (4 * n + g) * 32 + (c16 & 1) * 16) = vr[n];
    float m = -3.0e38f;
#pragma unroll
    for (int r = 0; r < 4; ++r) {
      if (4 * g + r > qi) sx0[r] = -3.0e38f;
      if (16 + 4 * g + r > qi) sx1[r] = -3.0e38f;
      m = fmaxf(m, fmaxf(sx0[r], sx1[r]));
    }
    { auto r = __builtin_amdgcn_permlane32_swap(ubits(m), ubits(m), false, false); m = fmaxf(fbits(r[0]), fbits(r[1])); }
    bf16x8 vx[8];
#pragma unroll
    for (int T = 0; T < 8; ++T) { const unsigned va = lb + T * 1024 + (4 * g + q_) * 32 + p_ * 8; vx[T] = cat8(tr_read(LP(va)), tr_read(LP(va + 512))); asm volatile("" : "+a"(vx[T])); }
    asm volatile("" : "+v"(sx0), "+v"(sx1), "+v"(m));
    if (seenM < (unsigned)s + 1) seenM = wait_ge(A, pM, s + 1, s < NSEG - 4 ? 1 : 2);
    asm volatile("" ::: "memory");
    TSTAMP(0, h);
    const unsigned mo = (unsigned)(s * NH + h) * 2048;
    const u32x4 km0 = bld16(rkm, l * 16, mo), km1 = bld16(rkm, l * 16 + 1024, mo);
    u32x4 vm[8];
#pragma unroll
    for (int T = 0; T < 8; ++T) { const u32x2 x = __builtin_amdgcn_raw_buffer_load_b64(rvm, c16 * 16 + (g & 1) * 8 + T * 256, mo, AUX_SC1); vm[T] = u32x4{x.x, x.y, 0u, 0u}; }
    asm volatile("" ::: "memory");
    *(LAS u32x4*)LP(lb + 8192 + l * 16) = km0; *(LAS u32x4*)LP(lb + 8192 + 1024 + l * 16) = km1;
#pragma unroll
    for (int ks = 0; ks < 4; ++ks) {
      const unsigned kr = lb + 8192 + (32 * ks + 8 * g + q_) * 16 + (p_ & 1) * 8;
      const bf16x8 kf = cat8(tr_read(LP(kr)), tr_read(LP(kr + 64)));
      if (ks == 0) MFV0(sm, kf, qf[0]); else MFV(sm, kf, qf[ks]);
    }
    asm volatile("s_nop 7" : "+v"(sm));
#pragma unroll
    for (int r = 0; r < 4; ++r) m = fmaxf(m, sm[r]);
    { auto r = __builtin_amdgcn_permlane16_swap(ubits(m), ubits(m), false, false); m = fmaxf(fbits(r[0]), fbits(r[1])); }
    const float mc = m * cexp, mcm = g < 2 ? mc : 1.0e30f;
    float pm[4], p0[4], p1[4];
#pragma unroll
    for (int r = 0; r < 4; ++r) {
      pm[r] = __builtin_amdgcn_exp2f(sm[r] * cexp - mcm); p0[r] = __builtin_amdgcn_exp2f(sx0[r] * cexp - mc); p1[r] = __builtin_amdgcn_exp2f(sx1[r] * cexp - mc);
    }
    u32x4 pmf = {pack2(pm[0], pm[1]), pack2(pm[2], pm[3]), 0u, 0u}, pxf = {pack2(p0[0], p0[1]), pack2(p0[2], p0[3]), pack2(p1[0], p1[1]), pack2(p1[2], p1[3])};
#pragma unroll
    for (int T = 0; T < 8; ++T) PIN1(vm[T]);
    asm volatile("s_nop 1" : "+v"(pmf), "+v"(pxf));
    f32x4 ls, o[8];
    MFV0(ls, ones8, pxf); MFV(ls, ones8, pmf);
#pragma unroll
    for (int T = 0; T < 8; ++T) { MFA0(o[T], vx[T], pxf); MFV(o[T], vm[T], pmf); }
    asm volatile("" : "+v"(ls));
    const float rl = __builtin_amdgcn_rcpf(ls[0]);
    const unsigned oo = ((unsigned)(s * 2 + qt) * 128 + h * 4) * 1024;
    u32x4 ov[4];
#pragma unroll
    for (int k = 0; k < 4; ++k) {
      if (k == 3) asm volatile("s_nop 7" : "+v"(o[6]), "+v"(o[7])); else asm volatile("" : "+v"(o[2 * k]), "+v"(o[2 * k + 1]));
      ov[k] = u32x4{pack2(o[2 * k][0] * rl, o[2 * k][1] * rl), pack2(o[2 * k][2] * rl, o[2 * k][3] * rl), pack2(o[2 * k + 1][0] * rl, o[2 * k + 1][1] * rl), pack2(o[2 * k + 1][2] * rl, o[2 * k + 1][3] * rl)};
      PIN1(ov[k]);
    }
#pragma unroll
    for (int k = 0; k < 4; ++k) bst16(ov[k], ro, l * 16 + k * 1024, oo);
    TSTAMP(2, h);
    vm_wait0();
    if (l == 0) st_flag(pA, s + 1, A.tag);
    TSTAMP(3, h);
  }
}

#define OSTAMP(i) do {} while (0)
DEV void out_role(const Args& A, const int o, ldsp L) {
  const int t = threadIdx.x, l = t & 63, g = l >> 4, c16 = l & 15, q_ = (l >> 2) & 3, p_ = l & 3;
  const int w = __builtin_amdgcn_readfirstlane(t >> 6), cg = o & 15, th = (o >> 4) & 1, par = o >> 5;
  const unsigned lb = (unsigned)(size_t)L;
  unsigned* ctrl = A.ctrl;
  bf16x8 wa[2][16], wb[2][7];
  {
    const unsigned off = (l >> 2) * (DM * 2) + (((l & 3) ^ (((l >> 4) & 1) << 1)) << 4);
    const unsigned mine = lb + w * 16384;
    const unsigned ra = mine + (4 * g + q_) * 64 + p_ * 8, rb = mine + (16 * (g >> 1) + 4 * (g & 1) + q_) * 64 + p_ * 8, sw = (g & 1) << 5;
#pragma unroll
    for (int half = 0; half < 2; ++half) {
#pragma unroll
      for (int n = 0; n < 16; ++n) glds(A.Wo + (size_t)(w * 512 + half * 256 + n * 16) * DM + cg * 32, off, mine + n * 1024);
      vm_wait0();
#pragma unroll
      for (int n = 0; n < 8; ++n)
#pragma unroll
        for (int c = 0; c < 2; ++c) { const unsigned x = ra + n * 2048 + ((c << 5) ^ sw); wa[c][half * 8 + n] = cat8(tr_read(LP(x)), tr_read(LP(x + 1024))); asm volatile("" : "+a"(wa[c][half * 8 + n])); }
      asm volatile("s_waitcnt lgkmcnt(0)" ::: "memory");
    }
#pragma unroll
    for (int n = 0; n < 14; ++n) glds(A.w2 + (size_t)(w * 224 + n * 16) * DM + cg * 32, off, mine + n * 1024);
    vm_wait0();
#pragma unroll
    for (int n = 0; n < 7; ++n)
#pragma unroll
      for (int c = 0; c < 2; ++c) { const unsigned x = rb + n * 2048 + ((c << 5) ^ sw); wb[c][n] = cat8(tr_read(LP(x)), tr_read(LP(x + 512))); PIN1(wb[c][n]); }
    raw_barrier();
  }
  const __amdgpu_buffer_rsrc_t ro = mk_rsrc(A.ws + WS_O, 16u << 20), rh = mk_rsrc(A.ws + WS_H, 8u << 20), rg = mk_rsrc(A.ws + WS_G, S_TOK * 4);
  unsigned seenH = 0;
  { (void)wait_ge(A, ctrl + C_PG + (l & 3) * 32, 1u, 1); (void)wait_ge(A, ctrl + C_PG + (4 + (l & 3)) * 32, 1u, 1); }
  unsigned* hp = ctrl + C_PP + (48 + ((l & 1) ? (7 * w + 6) / 8 : (7 * w) / 8)) * 32;
  for (int s = par; s < NSEG; s += 2) {
    OSTAMP(0);
    if (seenH < (unsigned)s + 1) seenH = wait_ge(A, hp, s + 1, 1);
    asm volatile("" ::: "memory");
    const unsigned rb = lb + ((s >> 1) & 1) * 32768;
    unsigned* ap = ctrl + C_PA + (((s & 3) * 8 + w) * 2 + th) * 32 + 2 * (l & 3);
    unsigned x = ld_flag(ap, A.tag);
    float gt;
    {
      u32x4 hf[7];
#pragma unroll
      for (int n = 0; n < 7; ++n) hf[n] = bld16(rh, l * 16 + (n & 3) * 1024, ((unsigned)(s * 2 + th) * 56 + w * 7 + (n & 4)) * 1024);
      gt = fbits(__builtin_amdgcn_raw_buffer_load_b32(rg, (s * 32 + th * 16 + ((l >> 2) & 15)) * 4, 0, AUX_SC1));
      f32x4 b0, b1;
#pragma unroll
      for (int n = 0; n < 7; ++n) { if (n == 0) { MFV0(b0, wb[0][0], hf[0]); MFV0(b1, wb[1][0], hf[0]); } else { MFV(b0, wb[0][n], hf[n]); MFV(b1, wb[1][n], hf[n]); } }
      asm volatile("s_nop 7" : "+v"(b0), "+v"(b1));
      *(LAS f32x4*)LP(rb + 16384 + w * 1024 + l * 16) = b0; *(LAS f32x4*)LP(rb + 24576 + w * 1024 + l * 16) = b1;
    }
    OSTAMP(1);
    for (int sp = 0; __any(x < (unsigned)s + 1); ++sp) {
      if (s < NSEG - 2) __builtin_amdgcn_s_sleep(3);
      if (give_up(A, sp)) break;
      if (l < 4) x = ld_flag(ap, A.tag); else x = ~0u;
    }
    asm volatile("" ::: "memory");
    {
      OSTAMP(2);
      f32x4 a0, a1;
      u32x4 of[16];
#define OF(n) bld16(ro, l * 16 + ((n) & 3) * 1024, ((unsigned)(s * 2 + th) * 128 + w * 16 + ((n) & 12)) * 1024)
#pragma unroll
      for (int n = 0; n < 10; ++n) of[n] = OF(n);
#pragma unroll
      for (int n = 0; n < 16; ++n) {
        if (n == 0) { MFA0(a0, wa[0][0], of[0]); MFA0(a1, wa[1][0], of[0]); } else { MFA(a0, wa[0][n], of[n]); MFA(a1, wa[1][n], of[n]); }
        if (n + 10 < 16) of[n + 10] = OF(n + 10);
      }
      asm volatile("s_nop 7" : "+v"(a0), "+v"(a1));
      OSTAMP(3);
      *(LAS f32x4*)LP(rb + w * 1024 + l * 16) = a0; *(LAS f32x4*)LP(rb + 8192 + w * 1024 + l * 16) = a1;
    }
    raw_barrier();
    OSTAMP(4);
    {
      float sa = 0.f, sb = 0.f;
      const unsigned e = rb + (w >> 2) * 8192 + (t & 255) * 4;
#pragma unroll
      for (int k = 0; k < 8; ++k) { sa += *(LAS float*)LP(e + k * 1024); sb += *(LAS float*)LP(e + 16384 + k * 1024); }
      const float og = (float)(bf16)(1.0f - gt);
      const float y = (float)(bf16)(gt * (float)(bf16)sa) + (float)(bf16)(og * (float)(bf16)sb);
      const float y1 = fbits(__builtin_amdgcn_update_dpp(0, ubits(y), 0xB1, 0xF, 0xF, true));
      if (!(l & 1)) *(unsigned*)(A.out + (size_t)(s * 32 + th * 16 + ((l >> 2) & 15)) * DM + cg * 32 + (w >> 2) * 16 + (w & 3) * 4 + (l & 2)) = pack2(y, y1);
    }
  }
}

#define NAP_O 900
#define NAP_Q 700
#define NAP_C 0
#define NAP_G 300
#define NAP_H 250
__global__ void __launch_bounds__(512, 2) k_main(Args A) {
  extern __shared__ __attribute__((aligned(16))) unsigned char lds_raw[];
  ldsp L = (ldsp)lds_raw;
  const int slot = get_slot(A.ctrl, L, A.tag);
  __syncthreads();
  const int x = slot >> 5, k = slot & 31;
  const bool noA = false, noO = false, noQ = false;
  if (x < 4) {
    if (k < 16) { nap(NAP_C); chain_role(A, x * 16 + k, L); }
    else {
      const int h = x * 8 + (k & 7);
      if (x == 0 && k >= 24 && k < 30) { calibrate(A, k - 24); __syncthreads(); } else nap(NAP_H);
      if (k < 24) warm(A.Wv + h * DH, HD * 2, (size_t)256 * HD * 2, L); else warm(A.Wo + (size_t)h * DH * DM, 256, 65536, L);
      init_lines(A, x * 16 + k - 16);
      if (k < 24 && !noA) attn_role(A, h, L);
    }
  } else {
    const int r = k < 8 ? (x - 4) * 8 + k : k * 4 + (x - 4);
    if (k >= 16 && k < 24) { const int h = (x - 4) * 8 + k - 16; warm(A.Wk + (size_t)256 * HD + h * DH, HD * 2, (size_t)((const char*)A.Wv - (const char*)A.Wk), L); }
    if (k == 24) warm(A.x + 32 * DM, 256, 65536, L);
    if (r >= 32 && r != 63) nap(r >= 64 ? NAP_O : r >= 55 ? NAP_G : NAP_Q);
    if (noQ && r >= 32 && r < 63) {}
    else if (r < 48) proj_role<0>(A, r, L);
    else if (r < 55) proj_role<1>(A, r, L);
    else if (r < 63) gate_role(A, r - 55);
    else if (r >= 64 && noO) {}
    else if (r >= 64) out_role(A, (x - 4) * 16 + k - 16, L);
    else if (threadIdx.x < 64) close_epoch(A);
  }
}

#define POLL_D1 12
#define POLL_D2 13
static int g_d1 = POLL_D1, g_d2 = POLL_D2;
static Args make_args(void* const* d_in, void* d_out, void* d_ws) {
  Args A;
  A.x = (const bf16*)d_in[0]; A.mem0 = (const bf16*)d_in[1]; A.Wq = (const bf16*)d_in[2]; A.Wk = (const bf16*)d_in[3]; A.Wv = (const bf16*)d_in[4]; A.Wo = (const bf16*)d_in[5];
  A.w1 = (const bf16*)d_in[8]; A.w2 = (const bf16*)d_in[9]; A.gw = (const bf16*)d_in[10];
  A.out = (bf16*)d_out;
  A.ctrl = (unsigned*)d_ws; A.tag = 0; A.pad0 = 0;
  A.ws = (unsigned char*)((((uintptr_t)d_ws + CTRL_BYTES) + ((2u << 20) - 1)) & ~(uintptr_t)((2u << 20) - 1));
  A.d1 = g_d1; A.d2 = g_d2;
  return A;
}
extern "C" void kernel_launch(void* const* d_in, const int* in_sizes, int n_in, void* d_out, int out_size, void* d_ws, size_t ws_size, hipStream_t stream) {
  Args A = make_args(d_in, d_out, d_ws);
  (void)hipFuncSetAttribute((const void*)k_main, hipFuncAttributeMaxDynamicSharedMemorySize, LDS_BYTES);
  k_main<<<256, 512, LDS_BYTES, stream>>>(A);
}
```
